```python
import torch

def _softmax(x, axis=-1):
    return torch.softmax(x, dim=axis)

def _gelu(x):
    return torch.nn.functional.gelu(x, approximate='tanh')

def _silu(x):
    return torch.nn.functional.silu(x)

def _sigmoid(x):
    return torch.sigmoid(x)

def _layernorm(x, axis=-1, eps=1e-05):
    m = x.mean(dim=axis, keepdim=True)
    v = x.var(dim=axis, keepdim=True, unbiased=False)
    return (x - m) / torch.sqrt(v + eps)

def _scatter_add(tgt, idx, src, axis=0):
    return tgt.clone().index_add_(axis, idx, src)

def _segment_sum(src, seg, n):
    out = torch.zeros((n,) + src.shape[1:], dtype=src.dtype)
    return out.index_add_(0, seg, src)

def _pad(x, pw):
    flat = tuple((v for pair in reversed(pw) for v in pair))
    return torch.nn.functional.pad(x, flat)
kernel_name = 'adaLN_cond__sliding_local_dilated__moe_arch_mod__prompt'

def setup_inputs():

    _w = lambda a: (lambda t: t.to(torch.bfloat16) if t.is_floating_point() else t)(torch.as_tensor(a))

    def _A_setup():
        B, S, D = (1, 2048, 640)
        return {'x': _w((lambda *s: torch.randn(*s, dtype=torch.bfloat16) * 0.05)(B, S, D)), 'wqkv': _w((lambda *s: torch.randn(*s, dtype=torch.bfloat16) * 0.05)(D, 3 * D)), 'wo': _w((lambda *s: torch.randn(*s, dtype=torch.bfloat16) * 0.05)(D, D))}
    _A_in = _A_setup()

    def _B_setup():
        B, S, D, F, C = (1, 2048, 640, 1728, 1)
        return {'x': _w((lambda *s: torch.randn(*s, dtype=torch.bfloat16) * 0.05)(B, S, D)), 'w_r': _w((lambda *s: torch.randn(*s, dtype=torch.bfloat16) * 3.2)(D)), 'w_gate': _w((lambda *s: torch.randn(*s, dtype=torch.bfloat16) * 0.1)(D, F)), 'w_up': _w((lambda *s: torch.randn(*s, dtype=torch.bfloat16) * 0.1)(D, F)), 'w_down': _w((lambda *s: torch.randn(*s, dtype=torch.bfloat16) * 0.1)(F, D))}
    _B_in = _B_setup()
    return {'A_x': _A_in['x'], 'A_wqkv': _A_in['wqkv'], 'A_wo': _A_in['wo'], 'B_x': _B_in['x'], 'B_w_r': _B_in['w_r'], 'B_w_gate': _B_in['w_gate'], 'B_w_up': _B_in['w_up'], 'B_w_down': _B_in['w_down'], 'c': _w((lambda *s: torch.randn(*s, dtype=torch.bfloat16) * 0.1)(1, 640) * 8.0), 'w_mod': _w((lambda *s: torch.randn(*s, dtype=torch.bfloat16) * 0.1)(640, 6 * 640) * 640 ** (-0.5) * 8.0)}

def reference(A_x, A_wqkv, A_wo, B_x, B_w_r, B_w_gate, B_w_up, B_w_down, c, w_mod):

    def _A_ref(x, wqkv, wo):
        B, S, D = x.shape
        H = 32
        Dh = D // H
        W, dil = (8, 2)
        qkv = (lambda x, *d: x.permute(*d))((x @ wqkv).reshape(B, S, 3, H, Dh), 2, 0, 3, 1, 4)
        q, k, v = (qkv[0], qkv[1], qkv[2])
        scale = Dh ** (-0.5)
        pos = torch.arange(S)
        off = torch.arange(W) * dil
        idx = pos[:, None] - off[None, :]
        mask = idx >= 0
        idx_c = torch.where(mask, idx, 0).reshape(-1)
        kg = k[:, :, idx_c, :].reshape(B, H, S, W, Dh)
        vg = v[:, :, idx_c, :].reshape(B, H, S, W, Dh)
        logits = torch.einsum('bhsd,bhswd->bhsw', q, kg) * scale
        logits = torch.where(mask[None, None, :, :], logits, -1000000000.0)
        attn = _softmax(logits, axis=-1)
        o = torch.einsum('bhsw,bhswd->bhsd', attn, vg)
        return (lambda x, *d: x.permute(*d))(o, 0, 2, 1, 3).reshape(B, S, D) @ wo

    def _B_ref(x, w_r, w_gate, w_up, w_down):
        B, S, D = x.shape
        C = 1
        r = x @ w_r
        idx = torch.argsort(-r, axis=-1)[:, :C]
        idx_e = (lambda x, n, axis: torch.repeat_interleave(x, n, dim=axis))(idx.reshape(B, C, 1), D, axis=2)
        x_sel = (lambda x, i, axis: torch.take_along_dim(x, i, dim=axis))(x, idx_e, axis=1)
        g = _silu(x_sel @ w_gate)
        u = x_sel @ w_up
        y = g * u @ w_down
        r_sel = _sigmoid((lambda x, i, axis: torch.take_along_dim(x, i, dim=axis))(r, idx, axis=1))
        y = y * r_sel.reshape(B, C, 1)
        b_off = (torch.arange(B) * S).reshape(B, 1)
        flat_idx = (idx + b_off).reshape(B * C)
        zero = (x * 0.0).reshape(B * S, D)
        upd = _scatter_add(zero, flat_idx, y.reshape(B * C, D), axis=0)
        return x + upd.reshape(B, S, D)
    x = A_x
    mod = (_silu(c) @ w_mod).reshape(c.shape[0], 6, 1, -1)
    sa, ca, ga, sb, cb, gb = (mod[:, 0].clone(), mod[:, 1].clone(), mod[:, 2].clone(), mod[:, 3].clone(), mod[:, 4].clone(), mod[:, 5].clone())
    h = _layernorm(x, axis=-1) * (1 + ca) + sa
    x = x + ga * _A_ref(h, A_wqkv, A_wo)
    h = _layernorm(x, axis=-1) * (1 + cb) + sb
    x = x + gb * _B_ref(h, B_w_r, B_w_gate, B_w_up, B_w_down)
    return x
```

```cpp
#include <hip/hip_runtime.h>
#include <stdint.h>

typedef __attribute__((ext_vector_type(8))) short bf16x8;
typedef __attribute__((ext_vector_type(4))) short s16x4;
typedef __attribute__((ext_vector_type(4))) float f32x4;
typedef __attribute__((ext_vector_type(2))) float f32x2;
typedef __attribute__((ext_vector_type(4))) unsigned u32x4;
typedef __attribute__((ext_vector_type(2))) unsigned u32x2;
typedef __attribute__((ext_vector_type(2))) __bf16 bf16x2;
typedef unsigned short u16;
typedef unsigned long long u64;
#define LAS __attribute__((address_space(3)))

#define HDL 16
#define GSL 2
#define YD 16
#define KN 30
#define YS 2
#define LZS 8
#define XS 5
#define PGI 9
#define O1 5
#define W0 12
#define H0 4
#define RN 1
#define RD 1
#define LA 10
#define SP 10
#define NS1 3
#define TH 10
#define WO1 18
#define TW 5
#define HS 17
#define HG 6

#define S_LEN 2048
#define DM 640
#define FF 1728
#define SC1 16
#define SPIN_CAP (1 << 18)
#define SENT 0xffffffffu
#define OOB 0x40000000

__device__ __forceinline__ float bf2f(unsigned v) { return __uint_as_float(v << 16); }
__device__ __forceinline__ unsigned pack2v(f32x2 v) {
  bf16x2 r = __builtin_convertvector(v, bf16x2);
  return __builtin_bit_cast(unsigned, r);
}
__device__ __forceinline__ unsigned pack2(float a, float b) { return pack2v((f32x2){a, b}); }
__device__ __forceinline__ float rbf(float f) { return __uint_as_float(pack2(f, 0.f) << 16); }
__device__ __forceinline__ f32x2 unpk(unsigned u) {
  f32x2 r;
  r.x = __uint_as_float(u << 16);
  r.y = __uint_as_float(u & 0xffff0000u);
  return r;
}
__device__ __forceinline__ f32x2 rbf2(f32x2 v) { return unpk(pack2v(v)); }
__device__ __forceinline__ unsigned nosent(unsigned u) { return u < 0xfffffffeu ? u : 0xfffffffeu; }
__device__ __forceinline__ unsigned umax(unsigned a, unsigned b) { return a > b ? a : b; }
__device__ __forceinline__ unsigned max4(u32x4 v, unsigned m) { return umax(umax(umax(v.x, v.y), v.z), umax(v.w, m)); }
__device__ __forceinline__ float dot2(unsigned a, unsigned b, float c) {
  return __builtin_amdgcn_fdot2_f32_bf16(__builtin_bit_cast(bf16x2, a), __builtin_bit_cast(bf16x2, b), c, false);
}

template <int CTRL> __device__ __forceinline__ float dppf(float v) {
  return __builtin_bit_cast(float, __builtin_amdgcn_update_dpp(0, __builtin_bit_cast(int, v), CTRL, 0xF, 0xF, true));
}
template <int CTRL> __device__ __forceinline__ unsigned dppu(unsigned v) {
  return (unsigned)__builtin_amdgcn_update_dpp(0, (int)v, CTRL, 0xF, 0xF, true);
}
__device__ __forceinline__ float swapsum16(float v) {
  auto r = __builtin_amdgcn_permlane16_swap(__float_as_uint(v), __float_as_uint(v), false, false);
  return __uint_as_float(r[0]) + __uint_as_float(r[1]);
}
__device__ __forceinline__ float swapsum32(float v) {
  auto r = __builtin_amdgcn_permlane32_swap(__float_as_uint(v), __float_as_uint(v), false, false);
  return __uint_as_float(r[0]) + __uint_as_float(r[1]);
}
__device__ __forceinline__ float sum64(float v) {
  v += dppf<0xB1>(v); v += dppf<0x4E>(v); v += dppf<0x141>(v); v += dppf<0x140>(v);
  return swapsum32(swapsum16(v));
}
__device__ __forceinline__ unsigned max64(unsigned v) {
  v = umax(v, dppu<0xB1>(v)); v = umax(v, dppu<0x4E>(v)); v = umax(v, dppu<0x141>(v)); v = umax(v, dppu<0x140>(v));
  auto r = __builtin_amdgcn_permlane16_swap(v, v, false, false);
  v = umax(r[0], r[1]);
  auto q = __builtin_amdgcn_permlane32_swap(v, v, false, false);
  return umax(q[0], q[1]);
}
typedef __attribute__((ext_vector_type(4))) short v4i16_t;
__device__ __forceinline__ s16x4 trread(const char* p) {
  return __builtin_bit_cast(s16x4, __builtin_amdgcn_ds_read_tr16_b64_v4i16((LAS v4i16_t*)p));
}
__device__ __forceinline__ u32x4 ld128(__amdgpu_buffer_rsrc_t rs, int off) {
  asm volatile("" : "+v"(off));
  return __builtin_amdgcn_raw_buffer_load_b128(rs, off, 0, SC1);
}
__device__ __forceinline__ u32x2 ld64(__amdgpu_buffer_rsrc_t rs, int off) {
  asm volatile("" : "+v"(off));
  return __builtin_amdgcn_raw_buffer_load_b64(rs, off, 0, SC1);
}
__device__ __forceinline__ unsigned ld32(__amdgpu_buffer_rsrc_t rs, int off) {
  asm volatile("" : "+v"(off));
  return __builtin_amdgcn_raw_buffer_load_b32(rs, off, 0, SC1);
}
#define ST128(v, off) __builtin_amdgcn_raw_buffer_store_b128((v), rs, (off), 0, SC1)
#define ST64(v, off) __builtin_amdgcn_raw_buffer_store_b64((v), rs, (off), 0, SC1)
#define ST32(v, off) __builtin_amdgcn_raw_buffer_store_b32((v), rs, (off), 0, SC1)
#define SBAR() __builtin_amdgcn_sched_barrier(0)
#define CFENCE() asm volatile("" ::: "memory")
#define LBAR() do { asm volatile("s_waitcnt lgkmcnt(0)" ::: "memory"); __builtin_amdgcn_s_barrier(); CFENCE(); } while (0)


#define LD128S(voff, soff) __builtin_amdgcn_raw_buffer_load_b128(rs, (voff), (soff), SC1)
#define DRAIN() asm volatile("s_waitcnt vmcnt(0)" ::: "memory")

#define WS_EPOCH 0
#define WS_FLAGP 4096
#define WS_FLAGW 8192
#define WS_FLAGA 16384
#define WS_FLAGO 20480
#define WS_FLAGH 40960
#define WS_MODF 69632
#define WS_MODP 90112
#define WS_WT 262144
#define WS_WOT (WS_WT + 16 * 20 * 7680)
#define WS_H (WS_WOT + 8 * 100 * 1024)
#define WS_O (WS_H + S_LEN * DM * 2)
#define WS_HALO (WS_O + S_LEN * DM * 2)
#define WS_FILL (WS_HALO + 256 * 5120)
#define WS_A WS_FILL
#define WS_BEST (WS_A + S_LEN * DM * 2)
#define WS_KEY (WS_BEST + 256 * 2560)
#define WS_YP (WS_KEY + 8 * 4096)
#define FILL_PER 14336
#define WS_END (WS_FILL + 256 * FILL_PER)
static_assert(WS_YP + 40 * 6912 <= WS_END, "fill region too small");
static_assert(WS_MODP + 4 * 30720 <= WS_WT, "control area");

#define L_MISC 0
#define L_CNT 256
#define L_MRED 512
#define L_S 5120
#define L_W 10240
#define L_XN 98304
#define L_PX 110592
#define KVS 248
#define L_KV (L_W - 16 * KVS)
#define L_XCH 10240
#define L_WO 53248
#define L_P3 155648
#define L_H2 L_S
#define L_U 7680
#define C_HL 16
#define C_HD 20
#define C_GO 19
#define C_FD 18
#define C_CH 17
__device__ __forceinline__ unsigned opaque(unsigned v) { asm("" : "+s"(v)); return v; }
#define ONE_LO opaque(0x00003f80u)
#define ONE_HI opaque(0x3f800000u)

__device__ __forceinline__ bool tag_ok(u32x4 g, unsigned tagl, unsigned tagh) {
  return ((((g.x ^ tagl) | (g.z ^ tagl)) & 0xffff0000u) | (g.y ^ tagh) | (g.w ^ tagh)) == 0u;
}
__device__ __forceinline__ float rs32(float a, float b) {
  auto r = __builtin_amdgcn_permlane32_swap(__float_as_uint(a), __float_as_uint(b), false, false);
  return __uint_as_float(r[0]) + __uint_as_float(r[1]);
}
__device__ __forceinline__ float rs16(float a, float b) {
  auto r = __builtin_amdgcn_permlane16_swap(__float_as_uint(a), __float_as_uint(b), false, false);
  return __uint_as_float(r[0]) + __uint_as_float(r[1]);
}
__device__ __forceinline__ float vmax(float a, float b) {
  float r;
  asm("v_max_f32 %0, %1, %2" : "=v"(r) : "v"(a), "v"(b));
  return r;
}
template <bool MAX> __device__ __forceinline__ void rows2(float& a, float& b) {
  auto r = __builtin_amdgcn_permlane16_swap(__float_as_uint(a), __float_as_uint(b), false, false);
  float t = MAX ? vmax(__uint_as_float(r[0]), __uint_as_float(r[1])) : __uint_as_float(r[0]) + __uint_as_float(r[1]);
  auto q = __builtin_amdgcn_permlane32_swap(__float_as_uint(t), __float_as_uint(t), false, false);
  t = MAX ? vmax(__uint_as_float(q[0]), __uint_as_float(q[1])) : __uint_as_float(q[0]) + __uint_as_float(q[1]);
  auto u = __builtin_amdgcn_permlane16_swap(__float_as_uint(t), __float_as_uint(t), false, false);
  a = __uint_as_float(u[0]); b = __uint_as_float(u[1]);
}
__device__ __forceinline__ unsigned padd(unsigned p, float al, float ah) { return pack2(dot2(p, ONE_LO, al), dot2(p, ONE_HI, ah)); }
__device__ __forceinline__ unsigned pmul(unsigned p, f32x2 m) { return pack2v(unpk(p) * m); }
__device__ __forceinline__ void wait_flags(__amdgpu_buffer_rsrc_t rs, int off, unsigned tagl, unsigned tagh) {
  int sp = 0;
  for (;;) {
    const u32x4 f = ld128(rs, off);
    if (__all(tag_ok(f, tagl, tagh)) || ++sp > SPIN_CAP) break;
    __builtin_amdgcn_s_sleep(2);
  }
  CFENCE();
}
__device__ __forceinline__ void wait_all256(__amdgpu_buffer_rsrc_t rs, int off, unsigned tagl, unsigned tagh) {
  int sp = 0;
  for (;;) {
    const u32x4 f0 = ld128(rs, off), f1 = ld128(rs, off + 1024);
    if (__all(tag_ok(f0, tagl, tagh) & tag_ok(f1, tagl, tagh)) || ++sp > SPIN_CAP) break;
    __builtin_amdgcn_s_sleep(8);
  }
  CFENCE();
}
__device__ __forceinline__ unsigned silu_lo(unsigned c) {
  const float cf = bf2f(c);
  return pack2(cf * __builtin_amdgcn_rcpf(1.0f + __expf(-cf)), 0.f);
}
template <int N> __device__ __forceinline__ void mod_partial(const u32x4 (&wm)[N], const unsigned (&sv)[N], char* lds, int w, int l) {
  float acc[8];
#pragma unroll
  for (int j = 0; j < 8; ++j) acc[j] = 0.f;
#pragma unroll
  for (int i = 0; i < N; ++i) {
    const unsigned sl = sv[i], sh = sl << 16;
#pragma unroll
    for (int j = 0; j < 4; ++j) {
      acc[2 * j] = dot2(wm[i][j], sl, acc[2 * j]);
      acc[2 * j + 1] = dot2(wm[i][j], sh, acc[2 * j + 1]);
    }
  }
  float v0 = rs32(acc[0], acc[4]), v1 = rs32(acc[1], acc[5]), v2 = rs32(acc[2], acc[6]), v3 = rs32(acc[3], acc[7]);
  v0 = rs16(v0, v2); v1 = rs16(v1, v3);
  v0 += dppf<0x128>(v0); v1 += dppf<0x128>(v1);
  *(f32x2*)(lds + L_MRED + w * 256 + (l & 7) * 32 + (l >> 4) * 8) = (f32x2){v0, v1};
}
template <int N> __device__ __forceinline__ float mod_sum(const char* lds, int l) {
  const float* src = (const float*)(lds + L_MRED) + l;
  const float a = (src[0] + src[64]) + (src[128] + src[192]);
  return N == 4 ? a : a + ((src[256] + src[320]) + (src[384] + src[448]));
}
template <int I> struct IC { static constexpr int v = I; };
template <int A, int B, class F> __device__ __forceinline__ void sfor(F&& f) {
  if constexpr (A + 1 == B) f(IC<A>{});
  else if constexpr (A < B) { sfor<A, (A + B) / 2>(f); sfor<(A + B) / 2, B>(f); }
}
template <int N> __device__ __forceinline__ void vmwait() { asm volatile("s_waitcnt vmcnt(%0)" ::"n"(N) : "memory"); }
#define MFMA(c, a, b) asm volatile("v_mfma_f32_16x16x32_bf16 %0, %1, %2, %0" : "+v"(c) : "v"(a), "v"(b))
#define MFMA0(c, a, b) asm volatile("v_mfma_f32_16x16x32_bf16 %0, %1, %2, 0" : "=&v"(c) : "v"(a), "v"(b))
#define LDX(d, voff, soff) asm volatile("buffer_load_dwordx4 %0, %1, %2, %3 offen sc1" : "=&v"(d) : "v"(voff), "s"(rs), "s"(soff) : "memory")
#define READY(d) asm volatile("" : "+v"(d))
#define LDXI(d, r, voff, soff) asm volatile("buffer_load_dwordx4 %0, %1, %2, %3 offen" : "=&v"(d) : "v"(voff), "s"(r), "s"(soff) : "memory")
#define LDXW(d, r, voff, soff) asm volatile("buffer_load_dword %0, %1, %2, %3 offen" : "=&v"(d) : "v"(voff), "s"(r), "s"(soff) : "memory")
#define LDXH(d, r, voff, soff) asm volatile("buffer_load_ushort %0, %1, %2, %3 offen" : "=&v"(d) : "v"(voff), "s"(r), "s"(soff) : "memory")
__device__ __forceinline__ void dma(__amdgpu_buffer_rsrc_t rs, int voff, int soff, unsigned ldsdst) {
  unsigned keep;
  asm volatile("s_mov_b32 %0, m0\n\ts_mov_b32 m0, %4\n\ts_nop 0\n\tbuffer_load_dwordx4 %1, %2, %3 offen sc1 lds\n\ts_mov_b32 m0, %0"
               : "=&s"(keep) : "v"(voff), "s"(rs), "s"(soff), "s"(ldsdst) : "memory");
}
__device__ __forceinline__ void wait_flag8(__amdgpu_buffer_rsrc_t rs, int off, u32x2 want) {
  unsigned n;
  u32x2 f;
  asm volatile("s_mov_b32 %1, %5\n"
               "1:\n\tbuffer_load_dwordx2 %0, %2, %3, 0 offen sc1\n\ts_waitcnt vmcnt(0)\n\tv_cmp_ne_u64 vcc, %4, %0\n\ts_cbranch_vccz 2f\n\t"
               "s_sleep 2\n\ts_sub_u32 %1, %1, 1\n\ts_cbranch_scc0 1b\n2:"
               : "=&v"(f), "=&s"(n) : "v"(off), "s"(rs), "s"(want), "n"(SPIN_CAP) : "vcc", "scc", "memory");
}
#define NCH 7
constexpr int cend(int c) { return c <= 0 ? 2 : c == 1 ? 4 : c == 2 ? 7 : c == 3 ? 10 : c == 4 ? 14 : c == 5 ? 17 : 20; }
constexpr int chunk_of(int s) { int c = 0; while (c < NCH - 1 && cend(c) <= s) ++c; return c; }
constexpr int needp(int c) { const int n = ((15 * cend(c) + 1) / 2 + 3) / 4; return n > 38 ? 38 : n; }
#define CODE_BYTES 42000
static_assert(SP >= 8 && SP < 15, "");
constexpr int hissued(int s) { const int n = H0 + RN * (s + 1) / RD; return n > 20 ? 20 : n; }
constexpr int hlast() { int s = 0; while (hissued(s) < 20) ++s; return s; }
#define ALL4 0x01010101u
__device__ __forceinline__ void lds_set(char* lds, int idx, int byte) { *((volatile LAS unsigned char*)(lds + L_CNT) + idx * 4 + byte) = 1; }
__device__ __forceinline__ unsigned lds_cnt(char* lds, int idx) { return *((volatile LAS unsigned*)(lds + L_CNT) + idx); }
__device__ __forceinline__ void lds_wait(char* lds, int idx, unsigned m) {
  int sp = 0;
  while ((__builtin_amdgcn_readfirstlane(lds_cnt(lds, idx)) & m) != m && ++sp < SPIN_CAP) __builtin_amdgcn_s_sleep(1);
  CFENCE();
}
__device__ __forceinline__ void row_stats(const unsigned (&p)[5], float& mb, float& isd) {
  float s = 0.f, q = 0.f;
#pragma unroll
  for (int i = 0; i < 5; ++i) { s = dot2(p[i], 0x3f803f80u, s); q = dot2(p[i], p[i], q); }
  {
    float v = rs32(s, q);
    v += dppf<0xB1>(v); v += dppf<0x4E>(v); v += dppf<0x141>(v); v += dppf<0x140>(v);
    v = swapsum16(v) * (1.0f / DM);
    s = __uint_as_float(__builtin_amdgcn_readlane(__float_as_uint(v), 0));
    q = __uint_as_float(__builtin_amdgcn_readlane(__float_as_uint(v), 32));
  }
  float var = q - s * s;
  if (__builtin_amdgcn_readfirstlane(s * s > 16.0f * var ? 1 : 0)) {
    float q2 = 0.f;
#pragma unroll
    for (int i = 0; i < 5; ++i) {
      const float d0 = dot2(p[i], ONE_LO, -s), d1 = dot2(p[i], ONE_HI, -s);
      q2 += d0 * d0; q2 += d1 * d1;
    }
    var = sum64(q2) * (1.0f / DM);
  }
  mb = rbf(s);
  isd = __builtin_amdgcn_rcpf(rbf(__builtin_amdgcn_sqrtf(rbf(rbf(var) + 1e-05f))));
}
__device__ __forceinline__ unsigned normalize(unsigned p, float mb, float isd) {
  return pack2v(rbf2(unpk(p) - (f32x2){mb, mb}) * (f32x2){isd, isd});
}

__global__ __launch_bounds__(512, 2) void k_all(const u16* __restrict__ x, const u16* __restrict__ wqkv,
                                                const u16* __restrict__ wo, const u16* __restrict__ wr,
                                                const u16* __restrict__ wg, const u16* __restrict__ wu,
                                                const u16* __restrict__ wd, const u16* __restrict__ cvec,
                                                const u16* __restrict__ wmod, char* ws, u16* __restrict__ out) {
  __shared__ __attribute__((aligned(256))) char lds[163840];
  asm volatile("" ::"s"(x), "s"(wqkv), "s"(wo), "s"(wr), "s"(wg), "s"(wu), "s"(wd), "s"(cvec), "s"(wmod), "s"(ws), "s"(out));
  int t = threadIdx.x, l = t & 63;
  const int w = __builtin_amdgcn_readfirstlane(t >> 6);
  const int b = blockIdx.x, xcd = b & 7, slot = b >> 3;
  int g4 = l >> 4, i16 = l & 15;
  const int mi = w ^ 4;
  const __amdgpu_buffer_rsrc_t rs = __builtin_amdgcn_make_buffer_rsrc(ws, 0, WS_END, 0x00020000);

  const int Lc = (b >> 5) * 8 + xcd, kq = (b >> 3) & 3;
  const bool urgent = Lc < 20, late = Lc >= 20 && Lc < 60;
  u32x4 wm[3];
  unsigned cv;
  const __amdgpu_buffer_rsrc_t rm = __builtin_amdgcn_make_buffer_rsrc((void*)wmod, 0, DM * 7680, 0x00020000);
  const __amdgpu_buffer_rsrc_t rcv = __builtin_amdgcn_make_buffer_rsrc((void*)cvec, 0, DM * 2, 0x00020000);
#define MOD_LOAD() do { \
    const int o = (l >> 3) * 7680 + (l & 7) * 16, so = (kq * 160 + w * 20) * 7680 + Lc * 128; \
    LDXH(cv, rcv, l < 20 ? l * 2 : OOB, (kq * 160 + w * 20) * 2); \
    LDXI(wm[0], rm, o, so); \
    LDXI(wm[1], rm, o, so + 8 * 7680); \
    LDXI(wm[2], rm, l < 32 ? o : OOB, so + 16 * 7680); \
  } while (0)
#define MOD_PARTIAL() do { \
    READY(cv); READY(wm[0]); READY(wm[1]); READY(wm[2]); \
    unsigned sv[3]; \
    const unsigned sl = silu_lo(cv); \
    _Pragma("unroll") for (int i = 0; i < 3; ++i) sv[i] = __builtin_amdgcn_ds_bpermute(((l >> 3) + 8 * i) * 4, sl); \
    mod_partial<3>(wm, sv, lds, w, l); \
  } while (0)
  if (urgent) MOD_LOAD();
  if (t < 64) *(unsigned*)(lds + L_CNT + t * 4) = 0u;
  u32x2 epv;
  asm volatile("buffer_load_dwordx2 %0, off, %1, 0 sc1" : "=&v"(epv) : "s"(rs) : "memory");
  SBAR();
  unsigned pfc;
  {
    const __amdgpu_buffer_rsrc_t rcode = __builtin_amdgcn_make_buffer_rsrc((void*)__builtin_amdgcn_s_getpc(), 0, CODE_BYTES, 0x00020000);
    LDXW(pfc, rcode, (w == 7 && l < 12) ? (slot * 12 + l) * 128 : OOB, 0);
  }
  SBAR();
  const int rb = (xcd >> 1) * 4 + (slot >> 3), hp = (xcd & 1) * 8 + (slot & 7);
  const int rg = ((xcd + 4) & 7) * 4 + (slot >> 3), cg = slot & 7;
  const int rc = ((xcd + 2) & 7) * 32 + slot;
  u32x4 rp[2], xv;
  int offA = l * 16, offB = 1024 + l * 4;
  unsigned xs[5];
  int rdst[2];
  {
    const __amdgpu_buffer_rsrc_t rw = __builtin_amdgcn_make_buffer_rsrc((void*)wqkv, 0, DM * 3840, 0x00020000);
    const int pc = 8 * (w & 1) + (l & 7);
    const int part = (pc * 13) >> 6;
    const int src = pc < 15 ? (l >> 3) * 3840 + pc * 16 + (part == 2 ? -160 : 1280 + part * 1200) : OOB;
    const int sb = rb * 40 * 3840 + hp * 80;
    LDXI(rp[0], rw, src, sb + (w >> 1) * 8 * 3840);
    const int nu = b >= 96 ? b - 80 : ((b - 64) >> 3) * 4 + (xcd - 4);
    const int Fp = nu * 5 + (w - 2);
    const bool wok = !urgent && w >= 2 && w < 7 && Fp < 800;
    const int kc = Fp / 10, ln = Fp - kc * 10, hq = kc / 5;
    const __amdgpu_buffer_rsrc_t r1 = __builtin_amdgcn_make_buffer_rsrc((void*)(w < 2 ? wqkv : wo), 0, w < 2 ? DM * 3840 : (wok ? DM * 1280 : 0), 0x00020000);
    LDXI(rp[1], r1, w < 2 ? src : (((l >> 3) & 3) + 20 * (l >> 5)) * 1280 + (l & 7) * 16,
         w < 2 ? sb + 32 * 3840 : (wok ? (hq * 40 + (kc - hq * 5) * 4) * 1280 + ln * 128 : 0));
    const __amdgpu_buffer_rsrc_t rx = __builtin_amdgcn_make_buffer_rsrc((void*)x, 0, S_LEN * DM * 2, 0x00020000);
    LDXI(xv, rx, offA, (rc * 8 + w) * 1280);
    LDXW(xs[4], rx, offB, (rc * 8 + w) * 1280);
    if (late) { __builtin_amdgcn_s_sleep(LZS); MOD_LOAD(); }
    SBAR();
    const int tt = 4 * (w & 1) + g4;
    const int kst = tt < 7 ? 256 : 128;
    const int lo7 = (tt == 7 && i16 >= 8) ? OOB : WS_WT + hp * 153600 + tt * 1024 + i16 * 16;
    const int kg0 = rb * 5 + (w >> 1), kg1 = rb * 5 + 4;
    rdst[0] = lo7 + (kg0 >> 2) * 7680 + (kg0 & 3) * kst;
    const int fo = ln < 8 ? ln * 102400 + g4 * 1024 : (4 * (ln - 8) + g4) * 102400 + 4096;
    rdst[1] = w < 2 ? lo7 + (kg1 >> 2) * 7680 + (kg1 & 3) * kst
                    : (wok ? WS_WOT + fo + (kc >> 2) * 5120 + (kc & 3) * 256 + i16 * 16 : OOB);
  }
  SBAR();
  int hdst = WS_H + ((rc >> 1) * 20 + (t >> 5)) * 1024 + (((t >> 3) & 3) * 16 + (rc & 1) * 8 + (t & 7)) * 16;
  int hdq;
  {
    const int chq = 64 + 2 * w + (l >> 5);
    hdq = WS_H + ((rc >> 1) * 20 + (chq >> 2)) * 1024 + ((chq & 3) * 16 + (rc & 1) * 8) * 16 + (l & 31) * 4;
  }
  SBAR();
#define EWAIT(n) do { if (late) vmwait<(n) + 4>(); else vmwait<(n)>(); } while (0)
  EWAIT(5);
  READY(epv);
  epv.x = __builtin_amdgcn_readfirstlane(epv.x); epv.y = __builtin_amdgcn_readfirstlane(epv.y);
  const u64 tag = ((((u64)epv.y << 32) | epv.x) + 1ull) & 0xffffffffffffull;
  const unsigned tagl = (unsigned)(tag << 16), tagh = (unsigned)(tag >> 16), tag32 = (unsigned)tag;
  const u32x2 flagv = {tagl | 1u, tagh};
  {
    float ms = 0.f;
    if (urgent) {
      MOD_PARTIAL();
      LBAR();
      if (w == 2) ms = mod_sum<8>(lds, l);
    }
    ST64(((u32x2){__float_as_uint(ms), tag32}), (urgent && w == 2) ? WS_MODP + kq * 30720 + (Lc * 64 + l) * 8 : OOB);
  }
  {
    EWAIT(3);
    READY(rp[0]); READY(rp[1]);
#pragma unroll
    for (int r = 0; r < 2; ++r) *(u32x4*)(lds + L_W + (w + 8 * r) * 1024 + ((l >> 1) & 3) * 256 + (l >> 3) * 32 + (l & 1) * 16) = rp[r];
    CFENCE();
    const char* stg = lds + L_W + w * 1024 + g4 * 256 + i16 * 8;
#pragma unroll
    for (int r = 0; r < 2; ++r) {
      const u32x2 lo = __builtin_bit_cast(u32x2, trread(stg + r * 8192)), hi = __builtin_bit_cast(u32x2, trread(stg + r * 8192 + 128));
      ST128(((u32x4){lo.x, lo.y, hi.x, hi.y}), rdst[r]);
    }
    CFENCE();
  }
  {
    EWAIT(3);
    READY(xv); READY(xs[4]);
    xs[0] = xv.x; xs[1] = xv.y; xs[2] = xv.z; xs[3] = xv.w;
    float mb, isd;
    row_stats(xs, mb, isd);
    char* xn = lds + L_XN + w * 1296;
    *(u32x4*)(xn + offA) = (u32x4){normalize(xs[0], mb, isd), normalize(xs[1], mb, isd), normalize(xs[2], mb, isd), normalize(xs[3], mb, isd)};
    *(unsigned*)(xn + offB) = normalize(xs[4], mb, isd);
  }
  u32x4 fw;
  {
    const int pc = t & 7;
    int goff = WS_MODP + ((pc >> 2) * DM + (t >> 3) * 8 + (pc & 3) * 2) * 8;
    int goff2 = WS_MODP + g4 * 30720 + ((pc >> 2) * DM + (64 + 2 * w + ((l >> 3) & 1)) * 8 + (pc & 3) * 2) * 8;
    int foff = w >= 4 ? WS_FLAGW + hp * 128 + (l & 7) * 16 : OOB;
    u32x4 g[4], g2;
    int fdst = t == 0 ? WS_FLAGW + (hp * 16 + rb) * 8 : (t == 1 ? WS_FLAGP + b * 8 : OOB);
    int xa0 = L_XN + (t & 7) * 1296 + (t >> 3) * 16, xa1 = L_XN + ((l >> 2) & 7) * 1296 + (64 + 2 * w + (l >> 5)) * 16 + (l & 3) * 4;
    u32x2 fv = flagv;
    asm volatile("" : "+v"(goff), "+v"(goff2), "+v"(foff), "+v"(fdst), "+v"(xa0), "+v"(xa1), "+v"(fv));
    DRAIN();
    asm volatile("" ::"v"(pfc));
    LBAR();
    ST64(fv, fdst);
#pragma unroll
    for (int q = 0; q < 4; ++q) g[q] = LD128S(goff, q * 30720);
    g2 = LD128S(goff2, 0);
    SBAR();
    const u32x4 xc = *(const u32x4*)(lds + xa0);
    const unsigned xq = *(const unsigned*)(lds + xa1);
    f32x2 xu[5] = {unpk(xc.x), unpk(xc.y), unpk(xc.z), unpk(xc.w), unpk(xq)};
    float one = (pc >> 2) ? 1.0f : 0.0f;
    int pw0 = L_PX + w * 640 + l * 8, pw1 = L_PX + w * 640 + 512 + (l & 15) * 8;
    int pr0 = L_PX + w * 640 + (l >> 3) * 64, pr1 = L_PX + w * 640 + 512 + (l >> 5) * 64 + (l & 3) * 8;
    asm volatile("" : "+v"(hdst), "+v"(hdq), "+v"(xu[0]), "+v"(xu[1]), "+v"(xu[2]), "+v"(xu[3]), "+v"(xu[4]), "+v"(one),
                 "+v"(pw0), "+v"(pw1), "+v"(pr0), "+v"(pr1));
    if (late) MOD_PARTIAL();
    int spins = 0;
    for (;;) {
      unsigned bad = (g2.y ^ tag32) | (g2.w ^ tag32);
#pragma unroll
      for (int q = 0; q < 4; ++q) bad |= (g[q].y ^ tag32) | (g[q].w ^ tag32);
      if (__all(bad == 0u) || ++spins > SPIN_CAP) break;
      __builtin_amdgcn_s_sleep(2);
      asm volatile("" : "+v"(goff), "+v"(goff2));
#pragma unroll
      for (int q = 0; q < 4; ++q) g[q] = LD128S(goff, q * 30720);
      g2 = LD128S(goff2, 0);
    }
    {
      const unsigned v = pack2((__uint_as_float(g[0].x) + __uint_as_float(g[1].x)) + (__uint_as_float(g[2].x) + __uint_as_float(g[3].x)),
                               (__uint_as_float(g[0].z) + __uint_as_float(g[1].z)) + (__uint_as_float(g[2].z) + __uint_as_float(g[3].z)));
      const unsigned v2 = pack2(swapsum32(swapsum16(__uint_as_float(g2.x))), swapsum32(swapsum16(__uint_as_float(g2.z))));
      *(f32x2*)(lds + pw0) = rbf2(unpk(v) + (f32x2){one, one});
      *(f32x2*)(lds + pw1) = rbf2(unpk(v2) + (f32x2){one, one});
    }
    CFENCE();
    const char* pr = lds + pr0;
    const f32x4 s0 = *(const f32x4*)(pr), s1 = *(const f32x4*)(pr + 16);
    const f32x4 c0 = *(const f32x4*)(pr + 32), c1 = *(const f32x4*)(pr + 48);
    const f32x2 sq = *(const f32x2*)(lds + pr1), cq = *(const f32x2*)(lds + pr1 + 32);
    u32x4 hv;
    hv.x = padd(pack2v(xu[0] * c0.xy), s0.x, s0.y);
    hv.y = padd(pack2v(xu[1] * c0.zw), s0.z, s0.w);
    hv.z = padd(pack2v(xu[2] * c1.xy), s1.x, s1.y);
    hv.w = padd(pack2v(xu[3] * c1.zw), s1.z, s1.w);
    ST128(hv, hdst);
    ST32(padd(pack2v(xu[4] * cq), sq.x, sq.y), hdq);
    fw = LD128S(foff, 0);
  }
  const int Ggu = (slot >> 2) * 8 + xcd;
  const bool is_gu = slot < 16 && Ggu < 27;
  const int fb = Ggu * 4 + (slot & 3);
  const bool is_dn = b >= 128 && b < 168;
  const int dnb = b - 128;
  {
    int fh = l == 0 ? WS_FLAGH + (rc >> 2) * 128 + (rc & 3) * 8 : OOB;
    asm volatile("" : "+v"(fh));
    unsigned cnt = l == 0 ? 1u : 0u, ca = (unsigned)(size_t)(LAS char*)lds + L_CNT + ((C_HD + l) & 63) * 4;
    asm volatile("" : "+v"(cnt), "+v"(ca));
    asm volatile("s_waitcnt vmcnt(1)\n\tds_add_rtn_u32 %0, %1, %0\n\ts_waitcnt lgkmcnt(0)" : "+v"(cnt) : "v"(ca) : "memory");
    ST64(flagv, cnt == 7u ? fh : OOB);
    asm volatile("" : "+v"(fw));
  }
  const int m2 = w & 3, kh = w >> 2;
  f32x4 mbias;
  int odst, odst4, kbo, vbo, qa[2][4];
  unsigned me;
#define ADDR_BLOCK() do { \
    const int lo = (rb == 0 && mi == 0) ? 8 : 0; \
    _Pragma("unroll") \
    for (int jj = 0; jj < 4; ++jj) { \
      const int kk = 4 * g4 + jj; \
      mbias[jj] = (kk >= lo && kk >= (i16 >> 1) + 1 && kk <= (i16 >> 1) + 8) ? 0.f : -1.0e30f; \
    } \
    const int mt = rb * 8 + mi, kc = 5 * hp + g4, kc4 = 5 * hp + 4; \
    odst = WS_O + (mt * 20 + (kc >> 2)) * 1024 + ((kc & 3) * 16 + i16) * 16; \
    odst4 = l < 16 ? WS_O + (mt * 20 + (kc4 >> 2)) * 1024 + ((kc4 & 3) * 16 + i16) * 16 : OOB; \
    kbo = L_KV + (16 * mi + 2 * i16) * KVS + g4 * 8; \
    vbo = L_KV + (16 * mi + 8 * g4 + 2 * ((l >> 2) & 3)) * KVS + 80 + (l & 3) * 8; \
      \
    const int qo = L_KV + (16 + 16 * mi + i16) * KVS + 160 + g4 * 8, zo = L_CNT + 128 + i16 * 8; \
    _Pragma("unroll") \
    for (int par = 0; par < 2; ++par) { \
      const bool pm = (i16 & 1) == par; \
      qa[par][0] = pm ? qo : zo; \
      qa[par][1] = (pm && g4 == 0) ? qo + 32 : zo; \
      qa[par][2] = (pm && g4 != 0) ? qo + 32 : zo; \
      qa[par][3] = (pm && g4 < 2) ? qo + 64 : zo; \
      asm volatile("" : "+v"(qa[par][0]), "+v"(qa[par][1]), "+v"(qa[par][2]), "+v"(qa[par][3])); \
    } \
    me = (i16 & 1) ? 0u : 0xffffffffu; \
    asm volatile("" : "+v"(odst), "+v"(odst4), "+v"(mbias), "+v"(kbo), "+v"(vbo), "+v"(me)); \
  } while (0)

  u32x4 wov[25];
  unsigned pfv[6];
  u32x4 hv[5];
  int hlo;
  if (w < 4) {
    const int hsrc = WS_H + (rb * 8 + 2 * w) * 20480;
    int wb = L_W + l * 16, wb7 = L_W + 7168 + ((l & 7) + 8 * g4) * 16;
    int kvo = L_KV + (16 + 32 * w + i16) * KVS + g4 * 8;
    int kvo7 = kvo + 224 - (g4 == 3 ? 8 : 0);
    asm volatile("" : "+v"(kvo), "+v"(kvo7), "+v"(wb), "+v"(wb7));
    ADDR_BLOCK();
    if (late && w == 2) {
      lds_wait(lds, C_HD, 8u);
      ST64(((u32x2){__float_as_uint(mod_sum<8>(lds, l)), tag32}), WS_MODP + kq * 30720 + (Lc * 64 + l) * 8);
    }
    __builtin_amdgcn_s_sleep(HDL);
    wait_flag8(rs, WS_FLAGH + (rb * 4 + w) * 128 + (l & 3) * 8, flagv);
    auto gemm = [&](auto SEQ) __attribute__((always_inline)) {
    f32x4 acc[2][8];
    u32x4 ha[20], hb[20];
#pragma unroll
    for (int s = 0; s < H0; ++s) { ha[s] = LD128S(l * 16, hsrc + s * 1024); hb[s] = LD128S(l * 16, hsrc + 20480 + s * 1024); }
    SBAR();
    lds_wait(lds, 0, ALL4);
    bf16x8 wf[9];
    unsigned rdy3;
    unsigned rdy = lds_cnt(lds, chunk_of(1)), rdys = __builtin_amdgcn_readfirstlane(rdy);
#pragma unroll
    for (int tt = 0; tt < 8; ++tt) wf[tt] = *(const bf16x8*)(lds + (tt < 7 ? wb + tt * 1024 : wb7));
    SBAR();
    auto hld = [&](auto K) __attribute__((always_inline)) {
      constexpr int k = decltype(K)::v;
      constexpr bool pair = decltype(SEQ)::v == 0 || k < 2 * SP;
      constexpr int mm = pair ? (k & 1) : (k < 20 + SP ? 1 : 0), ss = pair ? k / 2 : (k < 20 + SP ? k - SP : k - 20);
      if constexpr (mm) hb[ss] = LD128S(l * 16, hsrc + 20480 + ss * 1024); else ha[ss] = LD128S(l * 16, hsrc + ss * 1024);
    };
    auto step = [&](auto S) __attribute__((always_inline)) {
      constexpr int s = decltype(S)::v;
      constexpr int c0 = chunk_of(s), c1 = chunk_of(s + 1 < 20 ? s + 1 : s), c2 = chunk_of(s + 2 < 20 ? s + 2 : 19);
      if constexpr (c1 != c0) {
        if (__builtin_expect(rdys != ALL4, 0)) lds_wait(lds, c1, ALL4);
        CFENCE();
      }
      if constexpr (s == 19) {
        if (__builtin_expect(rdys != ALL4, 0)) lds_wait(lds, C_CH, ALL4);
        CFENCE();
      }
      const bf16x8 a = __builtin_bit_cast(bf16x8, ha[s]), bb = __builtin_bit_cast(bf16x8, hb[s]);
#pragma unroll
      for (int tt = 0; tt < 8; ++tt) {
        const int f = s * 8 + tt;
        if (s == 0) MFMA0(acc[0][tt], wf[f % 9], a); else MFMA(acc[0][tt], wf[f % 9], a);
        SBAR();
        if (s + 1 < 20) wf[(f + 8) % 9] = *(const bf16x8*)(lds + (tt < 7 ? wb + tt * 1024 : wb7) + ((s + 1) & 7) * 7680);
        if ((s & 7) == 6 && tt == 6) { wb += 8 * 7680; asm volatile("" : "+v"(wb)); }
        if ((s & 7) == 6 && tt == 7) { wb7 += 8 * 7680; asm volatile("" : "+v"(wb7)); }
        if (tt == 2 && c2 != c1) rdy = lds_cnt(lds, c2);
        if (tt == 6 && c2 != c1) rdys = __builtin_amdgcn_readfirstlane(rdy);
        if (s == 18 && tt == 2) rdy = lds_cnt(lds, C_CH);
        if (s == 18 && tt == 6) rdys = __builtin_amdgcn_readfirstlane(rdy);
        if (decltype(SEQ)::v == 1 && s == SP - 1 && tt == 3) rdy3 = lds_cnt(lds, C_CH) & lds_cnt(lds, NCH - 1);
        if (s == 19 && tt >= 2) *(u32x2*)(lds + kvo + (tt - 2) * 32) = (u32x2){pack2v(acc[0][tt - 2].xy), pack2v(acc[0][tt - 2].zw)};
        SBAR();
        if (s == 0) MFMA0(acc[1][tt], wf[f % 9], bb); else MFMA(acc[1][tt], wf[f % 9], bb);
        SBAR();
        {
          constexpr int n0 = hissued(s - 1), n1 = hissued(s);
          if constexpr (n1 > n0) {
            if (tt == 1) hld(IC<2 * n0>{});
            if (tt == 3) hld(IC<2 * n0 + 1>{});
          }
          if constexpr (n1 > n0 + 1) {
            if (tt == 5) hld(IC<2 * n0 + 2>{});
            if (tt == 7) hld(IC<2 * n0 + 3>{});
          }
        }
        if (s == 19 && tt >= 2) *(u32x2*)(lds + kvo + 16 * KVS + (tt - 2) * 32) = (u32x2){pack2v(acc[1][tt - 2].xy), pack2v(acc[1][tt - 2].zw)};
        SBAR();
      }
      if (decltype(SEQ)::v == 0 && s == hlast()) lds_set(lds, C_HL, w);
      if (s == 5) lds_set(lds, C_CH, w);
      if (s == HS) lds_set(lds, C_GO, w);
      SBAR();
    };
    sfor<0, SP>(step);
    if constexpr (decltype(SEQ)::v == 0) {
      sfor<SP, 20>(step);
      asm volatile("s_nop 7\n\ts_nop 1" ::: "memory");
      SBAR();
#pragma unroll
      for (int m = 0; m < 2; ++m) {
        *(u32x2*)(lds + kvo + m * 16 * KVS + 6 * 32) = (u32x2){pack2v(acc[m][6].xy), pack2v(acc[m][6].zw)};
        *(u32x2*)(lds + kvo7 + m * 16 * KVS) = (u32x2){pack2v(acc[m][7].xy), pack2v(acc[m][7].zw)};
      }
    } else {
      constexpr int N = 20 - SP, NI = hissued(SP - 1);
      if (__builtin_expect(__builtin_amdgcn_readfirstlane(rdy3) != ALL4, 0)) { lds_wait(lds, C_CH, ALL4); lds_wait(lds, NCH - 1, ALL4); }
      CFENCE();
      int wbb = wb + 8 * 7680, wb7b = wb7 + 8 * 7680, hdo = WS_HALO + (rb * 16 + hp) * 5120 + l * 16;
      asm volatile("" : "+v"(wbb), "+v"(wb7b), "+v"(hdo));
      auto frag = [&](int g) __attribute__((always_inline)) {
        const int sg = SP + (g < 5 * N ? g / 5 : (g - 5 * N) >> 3), tg = g < 5 * N ? g % 5 : (g - 5 * N) & 7;
        return *(const bf16x8*)(lds + (tg < 7 ? (sg < 16 ? wb : wbb) + tg * 1024 : (sg < 16 ? wb7 : wb7b)) + (sg & 7) * 7680);
      };
      bf16x8 wq[LA + 1];
#pragma unroll
      for (int g = 0; g < LA; ++g) wq[g] = g < 5 ? wf[(SP * 8 + g) % 9] : frag(g);
      SBAR();
      sfor<0, 16 * N>([&](auto F) {
        constexpr int f = decltype(F)::v, r = f - 5 * N, k = r % 11;
        constexpr int s = SP + (r < 0 ? f / 5 : r / 11), tt = r < 0 ? f % 5 : (k < 5 ? k : 5 + (k - 5) / 2);
        constexpr int m = r < 0 ? 1 : (k < 5 ? 0 : (k - 5) & 1), g = r < 0 ? f : 5 * N + 8 * (r / 11) + tt;
        constexpr bool first = r < 0 || k < 5 || m == 0;
        MFMA(acc[m][tt], wq[g % (LA + 1)], __builtin_bit_cast(bf16x8, m ? hb[s] : ha[s]));
        SBAR();
        if constexpr (first && g + LA < 13 * N) wq[(g + LA) % (LA + 1)] = frag(g + LA);
        if constexpr (f % HG == 1 && 2 * NI + f / HG < 40) hld(IC<2 * NI + f / HG>{});
        if constexpr (r >= 0 && r < 5) {
          const u32x2 pk = {pack2v(acc[1][r].xy), pack2v(acc[1][r].zw)};
          __builtin_amdgcn_raw_buffer_store_b128(((u32x4){pk.x, tag32, pk.y, tag32}), rs, hdo, r * 1024, SC1);
          *(u32x2*)(lds + kvo + r * 32 + 16 * KVS) = pk;
        }
        static_assert(NI < 20, "");
        if constexpr (f == HG * (40 - 2 * NI) - 2) lds_set(lds, C_HL, w);
        if constexpr (f >= 16 * N - 5) *(u32x2*)(lds + kvo + (f - 16 * N + 5) * 32) = (u32x2){pack2v(acc[0][f - 16 * N + 5].xy), pack2v(acc[0][f - 16 * N + 5].zw)};
        SBAR();
      });
      asm volatile("s_nop 7\n\ts_nop 1" ::: "memory");
      SBAR();
#pragma unroll
      for (int tt = 5; tt < 8; ++tt)
#pragma unroll
        for (int m = 0; m < 2; ++m)
          *(u32x2*)(lds + (tt < 7 ? kvo + tt * 32 : kvo7) + m * 16 * KVS) = (u32x2){pack2v(acc[m][tt].xy), pack2v(acc[m][tt].zw)};
    }
    };
    if (w < 3) gemm(IC<0>{}); else gemm(IC<1>{});
  } else {
    const int j = w - 4;
    const unsigned lbase = (unsigned)(size_t)(LAS char*)lds + L_W;
    const int wsrc = WS_WT + hp * 153600;
    if (!__all(tag_ok(fw, tagl, tagh))) wait_flags(rs, WS_FLAGW + hp * 128 + (l & 7) * 16, tagl, tagh);
    u32x4 fp[2];
    auto piece = [&](int i) __attribute__((always_inline)) {
      const int p = i < 37 ? j + 4 * i : (j < 2 ? j + 148 : 149);
      dma(rs, l * 16, wsrc + p * 1024, lbase + p * 1024);
    };
#pragma unroll
    for (int i = 0; i < W0; ++i) piece(i);
    ADDR_BLOCK();
    int flo = WS_FILL + b * FILL_PER + j * 1024 + l * 16, flo3 = j < 2 ? flo : OOB;
    unsigned fcnt = l == 0 ? 1u : 0u, fca = (unsigned)(size_t)(LAS char*)lds + L_CNT + ((C_FD + l) & 63) * 4;
    int fao = l == 0 ? WS_FLAGA + b * 8 : OOB;
    const int gq = g4 * 8 + xcd;
    int pfo = gq < 27 ? (slot * 20 + i16) * FF * 2 + gq * 128 : OOB, pfo1 = i16 < 4 ? pfo + 16 * FF * 2 : OOB;
    int pfo2 = gq < 27 ? (gq * 640 + slot * 20 + i16) * 128 : OOB, pfo3 = i16 < 4 ? pfo2 + 16 * 128 : OOB;
    asm volatile("" : "+v"(pfo1), "+v"(pfo2), "+v"(pfo3));
    hlo = rb > 0 ? WS_HALO + ((rb - 1) * 16 + hp) * 5120 + l * 16 : OOB;
    asm volatile("" : "+v"(flo), "+v"(flo3), "+v"(fcnt), "+v"(fca), "+v"(fao), "+v"(hlo), "+v"(pfo));
    static_assert(W0 >= TH && W0 <= 19 && TH <= 12, "");
    sfor<0, NCH>([&](auto C) {
      constexpr int c = decltype(C)::v;
      if constexpr (needp(c) <= W0 - TH) { vmwait<W0 - needp(c)>(); lds_set(lds, c, j); }
    });
    sfor<W0, 38>([&](auto I) {
      constexpr int i = decltype(I)::v, done = i - TH;
      vmwait<TH - 1>();
      sfor<0, NCH>([&](auto C) {
        constexpr int c = decltype(C)::v;
        if constexpr (done + 1 == needp(c)) { lds_set(lds, c, j); }
      });
      if constexpr (i == 19) lds_wait(lds, C_HD, 8u);
      if constexpr (i == 37 - TH) {
        LDX(fp[0], l * 16, WS_FLAGP);
        LDX(fp[1], l * 16, WS_FLAGP + 1024);
      }
      piece(i);
    });
    READY(fp[0]); READY(fp[1]);
#pragma unroll
    for (int i = 0; i < 4; ++i) __builtin_amdgcn_raw_buffer_store_b128(((u32x4){SENT, SENT, SENT, SENT}), rs, i < 3 ? flo : flo3, i * 4096, SC1);
    SBAR();
    sfor<0, NCH>([&](auto C) {
      constexpr int c = decltype(C)::v;
      if constexpr (needp(c) > 38 - TH) { vmwait<38 - needp(c) + 4>(); lds_set(lds, c, j); }
    });
    if (!__all(tag_ok(fp[0], tagl, tagh) & tag_ok(fp[1], tagl, tagh))) wait_all256(rs, WS_FLAGP + l * 16, tagl, tagh);
    asm volatile("s_waitcnt vmcnt(0)\n\tds_add_rtn_u32 %0, %1, %0\n\ts_waitcnt lgkmcnt(0)" : "+v"(fcnt) : "v"(fca) : "memory");
    ST64(flagv, fcnt == 3u ? fao : OOB);
#pragma unroll
    for (int i = 0; i < WO1; ++i) {
      vmwait<TW - 1>();
      LDX(wov[i], l * 16, WS_WOT + cg * 102400 + (j + 4 * i) * 1024);
    }
    lds_wait(lds, C_HL, ALL4);
#pragma unroll
    for (int i = WO1; i < 25; ++i) LDX(wov[i], l * 16, WS_WOT + cg * 102400 + (j + 4 * i) * 1024);
    if (j == 0) {
      lds_wait(lds, C_GO, 1u);
#pragma unroll
      for (int i = 0; i < 5; ++i) LDX(hv[i], hlo, i * 1024);
      const __amdgpu_buffer_rsrc_t rg0 = __builtin_amdgcn_make_buffer_rsrc((void*)wg, 0, DM * FF * 2, 0x00020000);
      const __amdgpu_buffer_rsrc_t rg1 = __builtin_amdgcn_make_buffer_rsrc((void*)wu, 0, DM * FF * 2, 0x00020000);
      const __amdgpu_buffer_rsrc_t rg2 = __builtin_amdgcn_make_buffer_rsrc((void*)wd, 0, DM * FF * 2, 0x00020000);
      LDXH(pfv[0], rg0, pfo, 0);
      LDXH(pfv[1], rg1, pfo, 0);
      LDXH(pfv[2], rg0, pfo1, 0);
      LDXH(pfv[3], rg1, pfo1, 0);
      LDXH(pfv[4], rg2, pfo2, 0);
      LDXH(pfv[5], rg2, pfo3, 0);
    }
  }
  asm volatile("" : "+v"(t));
  l = t & 63; g4 = l >> 4; i16 = l & 15;
  offA = l * 16; offB = 1024 + l * 4;
  LBAR();
  if (w == 4) {
    vmwait<6>();
#pragma unroll
    for (int i = 0; i < 5; ++i) READY(hv[i]);
    unsigned bad = 0u;
#pragma unroll
    for (int i = 0; i < 5; ++i) bad |= (hv[i].y ^ tag32) | (hv[i].w ^ tag32);
    if (__builtin_expect(rb > 0 && !__all(bad == 0u), 0)) {
      int sp = 0;
      do {
        __builtin_amdgcn_s_sleep(1);
#pragma unroll
        for (int i = 0; i < 5; ++i) LDX(hv[i], hlo, i * 1024);
        vmwait<0>();
        bad = 0u;
#pragma unroll
        for (int i = 0; i < 5; ++i) { READY(hv[i]); bad |= (hv[i].y ^ tag32) | (hv[i].w ^ tag32); }
      } while (!__all(bad == 0u) && ++sp < SPIN_CAP);
    }
#pragma unroll
    for (int i = 0; i < 5; ++i) *(u32x2*)(lds + L_KV + i16 * KVS + g4 * 8 + i * 32) = (u32x2){hv[i].x, hv[i].z};
    CFENCE();
  }
  u32x4 mw[5], dw[4];
  const __amdgpu_buffer_rsrc_t rgu = __builtin_amdgcn_make_buffer_rsrc((void*)(kh ? wu : wg), 0, DM * FF * 2, 0x00020000);
  const int guo = is_gu ? ((t & 255) >> 1) * (FF * 2) + fb * 32 + (t & 1) * 16 : OOB;
  SBAR();

  {
    const char* kb = lds + kbo;
    f32x4 sc[2] = {mbias, mbias};
    bf16x8 vf[2][2];
    {
      typedef __attribute__((ext_vector_type(6))) unsigned u32x6;
      u32x6 kk[2];
      u32x4 qb[2][2];
#pragma unroll
      for (int par = 0; par < 2; ++par) {
        const u32x2 k0 = *(const u32x2*)(kb + par * KVS), k1 = *(const u32x2*)(kb + par * KVS + 32), k2 = *(const u32x2*)(kb + par * KVS + 64);
        kk[par] = (u32x6){k0.x, k0.y, k1.x, k1.y, k2.x, k2.y};
#pragma unroll
        for (int h = 0; h < 2; ++h) {
          const u32x2 a = *(const u32x2*)(lds + qa[par][2 * h]), c = *(const u32x2*)(lds + qa[par][2 * h + 1]);
          qb[h][par] = (u32x4){a.x, a.y, c.x, c.y};
        }
      }
      const char* vb = lds + vbo;
#pragma unroll
      for (int h = 0; h < 2; ++h)
#pragma unroll
        for (int d = 0; d < 2; ++d) {
          const s16x4 lo = trread(vb + h * 40 + d * 32), hi = trread(vb + KVS + h * 40 + d * 32);
          vf[h][d] = __builtin_shufflevector(lo, hi, 0, 1, 2, 3, 4, 5, 6, 7);
        }
      SBAR();
      asm volatile("" : "+v"(kk[0]), "+v"(kk[1]));
#pragma unroll
      for (int par = 0; par < 2; ++par) {
        sc[0] = __builtin_amdgcn_mfma_f32_16x16x32_bf16(__builtin_bit_cast(bf16x8, __builtin_shufflevector(kk[par], kk[par], 0, 1, 2, 3)),
                                                        __builtin_bit_cast(bf16x8, qb[0][par]), sc[0], 0, 0, 0);
        sc[1] = __builtin_amdgcn_mfma_f32_16x16x32_bf16(__builtin_bit_cast(bf16x8, __builtin_shufflevector(kk[par], kk[par], 2, 3, 4, 5)),
                                                        __builtin_bit_cast(bf16x8, qb[1][par]), sc[1], 0, 0, 0);
      }
    }
    f32x4 od[2][2], lg[2];
    float mx[2], sm[2];
#pragma unroll
    for (int h = 0; h < 2; ++h) {
      const f32x2 sc2 = {0.22360679774997896f, 0.22360679774997896f};
      const f32x2 a = rbf2(rbf2(sc[h].xy) * sc2), c = rbf2(rbf2(sc[h].zw) * sc2);
      lg[h] = (f32x4){a.x, a.y, c.x, c.y};
      mx[h] = fmaxf(fmaxf(a.x, a.y), fmaxf(c.x, c.y));
    }
    rows2<true>(mx[0], mx[1]);
#pragma unroll
    for (int h = 0; h < 2; ++h) {
      const float k2 = 1.4426950408889634f;
      const float nm = mx[h] * -k2;
#pragma unroll
      for (int j = 0; j < 4; ++j) lg[h][j] = __builtin_amdgcn_exp2f(__builtin_fmaf(lg[h][j], k2, nm));
      sm[h] = (lg[h][0] + lg[h][1]) + (lg[h][2] + lg[h][3]);
    }
    rows2<false>(sm[0], sm[1]);
#pragma unroll
    for (int h = 0; h < 2; ++h) {
      const float inv = __builtin_amdgcn_rcpf(sm[h]);
      const unsigned p01 = pack2(lg[h][0] * inv, lg[h][1] * inv), p23 = pack2(lg[h][2] * inv, lg[h][3] * inv);
      const bf16x8 pf = __builtin_bit_cast(bf16x8, ((u32x4){p01 & me, p23 & me, p01 & ~me, p23 & ~me}));
#pragma unroll
      for (int d = 0; d < 2; ++d)
        od[h][d] = __builtin_amdgcn_mfma_f32_16x16x32_bf16(vf[h][d], pf, (f32x4){0.f, 0.f, 0.f, 0.f}, 0, 0, 0);
    }
    ST128(((u32x4){pack2v(od[0][0].xy), pack2v(od[0][0].zw), pack2v(od[1][0].xy), pack2v(od[1][0].zw)}), odst);
    ST128(((u32x4){pack2v(od[0][1].xy), pack2v(od[0][1].zw), pack2v(od[1][1].xy), pack2v(od[1][1].zw)}), odst4);
    SBAR();
    int woa = L_WO + (w - 4) * 1024 + l * 16, wob = woa + 13 * 4096;
    asm volatile("" : "+v"(woa), "+v"(wob));
    if (w >= 4) {
      vmwait<14>();
#pragma unroll
      for (int i = 0; i < 13; ++i) READY(wov[i]);
#pragma unroll
      for (int i = 0; i < 13; ++i) *(u32x4*)(lds + woa + i * 4096) = wov[i];
    }
    int fo = l == 0 ? WS_FLAGO + (rb * 8 + mi) * 128 + hp * 8 : OOB;
    asm volatile("" : "+v"(fo));
    DRAIN();
    asm volatile("" ::"v"(pfv[0]), "v"(pfv[1]), "v"(pfv[2]), "v"(pfv[3]), "v"(pfv[4]), "v"(pfv[5]));
    ST64(flagv, fo);
    if (w >= 4) {
#pragma unroll
      for (int i = 13; i < 25; ++i) READY(wov[i]);
#pragma unroll
      for (int i = 13; i < 25; ++i) *(u32x4*)(lds + wob + (i - 13) * 4096) = wov[i];
    }
  }
  if (w == 3 && kq == 0 && late) {
    u32x2 g[4];
    int sp = 0;
    for (;;) {
      unsigned bad = 0u;
#pragma unroll
      for (int q = 0; q < 4; ++q) g[q] = ld64(rs, WS_MODP + q * 30720 + (Lc * 64 + l) * 8);
#pragma unroll
      for (int q = 0; q < 4; ++q) bad |= g[q].y ^ tag32;
      if (__all(bad == 0u) || ++sp > SPIN_CAP) break;
      __builtin_amdgcn_s_sleep(4);
    }
    float s = rbf((__uint_as_float(g[0].x) + __uint_as_float(g[1].x)) + (__uint_as_float(g[2].x) + __uint_as_float(g[3].x)));
    if (Lc >= 40 && Lc < 50) s = rbf(1.0f + s);
    ST64(((u32x2){tagl | (__float_as_uint(s) >> 16), tagh}), WS_MODF + ((Lc - 20) * 64 + l) * 8);
  }
  int wqo = L_WO + kh * 50 * 1024 + l * 16, ofo = WS_FLAGO + (rg * 4 + m2) * 128 + kh * 64 + (l & 7) * 8;
  int osrc = WS_O + ((rg * 4 + m2) * 20 + kh * 10) * 1024;
  asm volatile("" : "+v"(wqo), "+v"(ofo), "+s"(osrc));
  LBAR();

  f32x2 xf[5];
  {
    const char* wb = lds + wqo;
    bf16x8 wq[25];
    wait_flag8(rs, ofo, flagv);
    u32x4 of[10], pg[5];
    unsigned wrp[2];
#pragma unroll
    for (int i = 0; i < O1; ++i) { of[i] = LD128S(offA, osrc + i * 1024); SBAR(); }
    SBAR();
#pragma unroll
    for (int i = 0; i < 25; ++i) wq[i] = *(const bf16x8*)(wb + i * 1024);
    int xwo = L_XCH + (kh * 64 + m2 * 16 + i16) * 336 + g4 * 16;
    int xr0 = L_XCH + (t >> 3) * 336 + (t & 7) * 32, xr1 = L_XCH + (t >> 3) * 336 + 256 + (t & 7) * 8;
    int ad0 = WS_A + (rg * 64 + (t >> 3)) * 1024 + cg * 128 + (t & 7) * 16, ad1 = WS_A + S_LEN * 1024 + (cg * S_LEN + rg * 64) * 32 + t * 4;
    int pgo = w < 4 ? WS_MODF + t * 16 : OOB, pgo4 = w < 4 ? WS_MODF + (w * 320 + 256 + l) * 16 : OOB;
    asm volatile("" : "+v"(xwo), "+v"(xr0), "+v"(xr1), "+v"(ad0), "+v"(ad1), "+v"(pgo), "+v"(pgo4));
    SBAR();
#pragma unroll
    for (int i = O1; i < 10; ++i) { of[i] = LD128S(offA, osrc + i * 1024); SBAR(); }
    SBAR();
    u32x4 fa0, fa1;
    if (w == 3) { fa0 = ld128(rs, WS_FLAGA + l * 16); fa1 = ld128(rs, WS_FLAGA + 1024 + l * 16); }
    SBAR();
    f32x4 a2[5];
#pragma unroll
    for (int i = 0; i < 10; ++i) {
      const bf16x8 ofr = __builtin_bit_cast(bf16x8, of[i]);
#pragma unroll
      for (int n = 0; n < 5; ++n) {
        const int f = i * 5 + n;
        if (i == 0) MFMA0(a2[n], wq[f % 25], ofr); else MFMA(a2[n], wq[f % 25], ofr);
        SBAR();
        if (f < 25) wq[f] = *(const bf16x8*)(wb + (25 + f) * 1024);
        SBAR();
      }
      if (i == PGI) {
#pragma unroll
        for (int j = 0; j < 4; ++j) pg[j] = LD128S(pgo, j * 5120);
        pg[4] = LD128S(pgo4, 0);
        const __amdgpu_buffer_rsrc_t rwr = __builtin_amdgcn_make_buffer_rsrc((void*)wr, 0, w < 4 ? DM * 2 : 0, 0x00020000);
        wrp[0] = __builtin_amdgcn_raw_buffer_load_b32(rwr, t * 4, 0, 0);
        wrp[1] = __builtin_amdgcn_raw_buffer_load_b32(rwr, offB, 0, 0);
      }
      if (i > XS && i <= XS + NS1) mw[i - XS - 1] = __builtin_amdgcn_raw_buffer_load_b128(rgu, guo, (i - XS - 1) * 128 * FF * 2, 0);
      SBAR();
    }
    asm volatile("s_nop 7\n\ts_nop 1" ::: "memory");
    SBAR();
#pragma unroll
    for (int n = 0; n < 5; ++n) *(f32x4*)(lds + xwo + n * 64) = a2[n];
    if (w == 3 && !__all(tag_ok(fa0, tagl, tagh) & tag_ok(fa1, tagl, tagh))) wait_all256(rs, WS_FLAGA + l * 16, tagl, tagh);
    LBAR();
    {
      const f32x4 s0 = *(const f32x4*)(lds + xr0) + *(const f32x4*)(lds + xr0 + 64 * 336);
      const f32x4 s1 = *(const f32x4*)(lds + xr0 + 16) + *(const f32x4*)(lds + xr0 + 64 * 336 + 16);
      const f32x2 s2 = *(const f32x2*)(lds + xr1) + *(const f32x2*)(lds + xr1 + 64 * 336);
      ST128(((u32x4){nosent(pack2v(s0.xy)), nosent(pack2v(s0.zw)), nosent(pack2v(s1.xy)), nosent(pack2v(s1.zw))}), ad0);
      ST32(nosent(pack2v(s2)), ad1);
    }
    if (w < 4) {
#define PG_OK() __all(tag_ok(pg[0], tagl, tagh) & tag_ok(pg[1], tagl, tagh) & tag_ok(pg[2], tagl, tagh) & tag_ok(pg[3], tagl, tagh) & tag_ok(pg[4], tagl, tagh))
      if (__builtin_expect(!PG_OK(), 0)) {
        int spins = 0;
        do {
          __builtin_amdgcn_s_sleep(4);
#pragma unroll
          for (int j = 0; j < 4; ++j) pg[j] = ld128(rs, pgo + j * 5120);
          pg[4] = ld128(rs, pgo4);
        } while (!PG_OK() && ++spins < SPIN_CAP);
      }
      unsigned pp[5];
#pragma unroll
      for (int j = 0; j < 5; ++j) pp[j] = (pg[j].x & 0xffffu) | (pg[j].z << 16);
#pragma unroll
      for (int j = 0; j < 4; ++j) *(unsigned*)(lds + L_P3 + j * 1280 + t * 4) = pp[j];
      *(unsigned*)(lds + L_P3 + w * 1280 + offB) = pp[4];
      *(unsigned*)(lds + L_P3 + 4 * 1280 + t * 4) = wrp[0];
      {
        const f32x2 u = unpk(pp[2]) * unpk(wrp[0]);
        const unsigned uh = pack2v(u);
        *(unsigned*)(lds + L_U + t * 4) = uh;
        *(unsigned*)(lds + L_U + 1280 + t * 4) = pack2v(u - unpk(uh));
      }
      if (w == 2) {
        const f32x2 u = unpk(pp[4]) * unpk(wrp[1]);
        const unsigned uh = pack2v(u);
        *(unsigned*)(lds + L_U + offB) = uh;
        *(unsigned*)(lds + L_U + 1280 + offB) = pack2v(u - unpk(uh));
        *(unsigned*)(lds + L_P3 + 4 * 1280 + offB) = wrp[1];
      }
    }
    LBAR();
  }

  const int row3 = rc * 8 + w;
  const __amdgpu_buffer_rsrc_t ro = __builtin_amdgcn_make_buffer_rsrc((void*)out, 0, S_LEN * DM * 2, 0x00020000);
  unsigned x1[5], hq[5];
  f32x2 gbf[5], sbf[5], cbf[5];
  float mb, isd;
  unsigned key;
  int kdst;
  {
    const int asrc = WS_A + row3 * 1024 + l * 16, asrc2 = WS_A + S_LEN * 1024 + ((l >> 3) * S_LEN + row3) * 32 + (l & 7) * 4;
    u32x4 av = ld128(rs, asrc);
    unsigned av2 = ld32(rs, asrc2);
    SBAR();
    unsigned pr[5][5];
#pragma unroll
    for (int a = 0; a < 5; ++a) {
      const u32x4 v = *(const u32x4*)(lds + L_P3 + a * 1280 + offA);
      pr[a][0] = v.x; pr[a][1] = v.y; pr[a][2] = v.z; pr[a][3] = v.w;
      pr[a][4] = *(const unsigned*)(lds + L_P3 + a * 1280 + offB);
    }
    kdst = l < 8 ? WS_KEY + l * 4096 + rc * 16 : OOB;
    asm volatile("" : "+v"(kdst));
    unsigned gal[5], gah[5], uh[5], ul[5];
    {
      const u32x4 v = *(const u32x4*)(lds + L_U + offA), v2 = *(const u32x4*)(lds + L_U + 1280 + offA);
      uh[0] = v.x; uh[1] = v.y; uh[2] = v.z; uh[3] = v.w; uh[4] = *(const unsigned*)(lds + L_U + offB);
      ul[0] = v2.x; ul[1] = v2.y; ul[2] = v2.z; ul[3] = v2.w; ul[4] = *(const unsigned*)(lds + L_U + 1280 + offB);
    }
    float U = 0.f;
#pragma unroll
    for (int i = 0; i < 5; ++i) {
      gal[i] = pr[0][i] & 0xffffu; gah[i] = pr[0][i] & 0xffff0000u;
      U = dot2(pr[2][i], pr[4][i], U);
      asm volatile("" : "+v"(gal[i]), "+v"(gah[i]), "+v"(uh[i]), "+v"(ul[i]));
    }
    asm volatile("" : "+v"(U));
#pragma unroll
    for (int i = 0; i < 5; ++i) { xf[i] = unpk(xs[i]); asm volatile("" : "+v"(xf[i])); }
#pragma unroll
    for (int i = 0; i < 5; ++i) {
      sbf[i] = unpk(pr[1][i]); cbf[i] = unpk(pr[2][i]); gbf[i] = unpk(pr[3][i]);
      asm volatile("" : "+v"(sbf[i]), "+v"(cbf[i]), "+v"(gbf[i]));
    }
    int spins = 0;
    for (;;) {
      if (__all(max4(av, av2) != SENT) || ++spins > SPIN_CAP) break;
      __builtin_amdgcn_s_sleep(2);
      av = ld128(rs, asrc);
      av2 = ld32(rs, asrc2);
    }
    const unsigned aa[5] = {av.x, av.y, av.z, av.w, av2};
    float s = 0.f, q = 0.f, d0 = 0.f, d1 = 0.f;
#pragma unroll
    for (int i = 0; i < 5; ++i) {
      x1[i] = padd(pack2(dot2(aa[i], gal[i], 0.f), dot2(aa[i], gah[i], 0.f)), xf[i].x, xf[i].y);
      s = dot2(x1[i], 0x3f803f80u, s); q = dot2(x1[i], x1[i], q);
      d0 = dot2(x1[i], uh[i], d0); d1 = dot2(x1[i], ul[i], d1);
    }
    {
      float v = rs16(rs32(s, q), rs32(d0 + d1, U));
      v += dppf<0xB1>(v); v += dppf<0x4E>(v); v += dppf<0x141>(v); v += dppf<0x140>(v);
      s = __uint_as_float(__builtin_amdgcn_readlane(__float_as_uint(v), 0)) * (1.0f / DM);
      q = __uint_as_float(__builtin_amdgcn_readlane(__float_as_uint(v), 32)) * (1.0f / DM);
      d0 = __uint_as_float(__builtin_amdgcn_readlane(__float_as_uint(v), 16));
      U = __uint_as_float(__builtin_amdgcn_readlane(__float_as_uint(v), 48));
    }
    float var = q - s * s;
    if (__builtin_expect(__builtin_amdgcn_readfirstlane(s * s > 16.0f * var ? 1 : 0), 0)) {
      float q2 = 0.f;
#pragma unroll
      for (int i = 0; i < 5; ++i) {
        const float e0 = dot2(x1[i], ONE_LO, -s), e1 = dot2(x1[i], ONE_HI, -s);
        q2 += e0 * e0; q2 += e1 * e1;
      }
      var = sum64(q2) * (1.0f / DM);
    }
    mb = rbf(s);
    isd = __builtin_amdgcn_rcpf(rbf(__builtin_amdgcn_sqrtf(rbf(rbf(var) + 1e-05f))));
    {
      const float r = (d0 - mb * U) * isd;
      const unsigned u = __float_as_uint(r);
      unsigned ord = u ^ ((unsigned)((int)u >> 31) | 0x80000000u);
      if (r != r) ord = 0u;
      key = (ord & ~2047u) | (unsigned)(2047 - row3);
    }
    *(unsigned*)(lds + L_MISC + 128 + w * 4) = key;
  }
  LBAR();
  bool best;
  {
    const u32x4 ka = *(const u32x4*)(lds + L_MISC + 128), kb = *(const u32x4*)(lds + L_MISC + 144);
    best = __builtin_amdgcn_readfirstlane(max4(ka, max4(kb, 0u)) == key ? 1 : 0) != 0;
  }
  ST32(key, best ? kdst : OOB);
  SBAR();
#define S2_LOADS() do { \
    _Pragma("unroll") for (int i = NS1; i < 5; ++i) mw[i] = __builtin_amdgcn_raw_buffer_load_b128(rgu, guo, i * 128 * FF * 2, 0); \
    SBAR(); \
  } while (0)
#define HQ() do { _Pragma("unroll") for (int i = 0; i < 5; ++i) hq[i] = padd(pmul(normalize(x1[i], mb, isd), cbf[i]), sbf[i].x, sbf[i].y); } while (0)
  if (best) {
    __builtin_amdgcn_s_setprio(2);
    HQ();
    ST128(((u32x4){nosent(hq[0]), nosent(hq[1]), nosent(hq[2]), nosent(hq[3])}), WS_BEST + rc * 2560 + offA);
    ST32(nosent(hq[4]), WS_BEST + rc * 2560 + offB);
    __builtin_amdgcn_s_setprio(0);
    SBAR();
    S2_LOADS();
  } else {
    __builtin_amdgcn_s_sleep(GSL);
    S2_LOADS();
    HQ();
  }
  unsigned oq[5];
#pragma unroll
  for (int i = 0; i < 5; ++i) {
    const f32x2 e = unpk(x1[i]);
    oq[i] = padd(pmul(hq[i], gbf[i]), e.x, e.y);
  }
#define OUT_ROW() do { \
    __builtin_amdgcn_raw_buffer_store_b128(((u32x4){oq[0], oq[1], oq[2], oq[3]}), ro, row3 * 1280 + offA, 0, SC1); \
    __builtin_amdgcn_raw_buffer_store_b32(oq[4], ro, row3 * 1280 + offB, 0, SC1); \
  } while (0)
#define X1_PUB() do { \
    DRAIN(); \
    ST128(((u32x4){nosent(x1[0]), nosent(x1[1]), nosent(x1[2]), nosent(x1[3])}), WS_BEST + rc * 2560 + 1280 + offA); \
    ST32(nosent(x1[4]), WS_BEST + rc * 2560 + 1280 + offB); \
  } while (0)
  if (best || !is_gu) OUT_ROW();
  if (best && !is_gu) X1_PUB();

  if (is_gu) {
    unsigned kmax;
    float z0 = 0.f, z1 = 0.f;
    {
      {
    const __amdgpu_buffer_rsrc_t rd = __builtin_amdgcn_make_buffer_rsrc((void*)wd, 0, FF * 1280, 0x00020000);
    const int o = (is_gu && w < 5) ? (fb * 16 + g4 * 4) * 1280 + w * 256 + i16 * 16 : OOB;
#pragma unroll
    for (int i = 0; i < 4; ++i) dw[i] = __builtin_amdgcn_raw_buffer_load_b128(rd, o, i * 1280, 0);
      }
      SBAR();
      bf16x8 af[5];
      unsigned kk[4];
      int ko = w == 7 ? WS_KEY + xcd * 4096 + l * 16 : OOB;
#define KEY_LOAD() do { SBAR(); asm volatile("" : "+v"(ko)); _Pragma("unroll") for (int i = 0; i < 4; ++i) kk[i] = __builtin_amdgcn_raw_buffer_load_b32(rs, ko, i * 1024, SC1); SBAR(); } while (0)
      unsigned dp[2][8];
      int hbo = L_H2 + (w & 3) * 64 + g4 * 16, mro = i16 == 0 ? L_MRED + w * 64 + g4 * 16 : L_MRED + 1024;
      {
        char* stg = lds + L_WO + w * 5120;
#pragma unroll
        for (int i = 0; i < 5; ++i) *(u32x4*)(stg + i * 1024 + l * 16) = mw[i];
        CFENCE();
#pragma unroll
        for (int i = 0; i < 5; ++i) {
          const s16x4 lo = trread(stg + i * 1024 + g4 * 256 + i16 * 8), hi = trread(stg + i * 1024 + g4 * 256 + i16 * 8 + 128);
          af[i] = __builtin_shufflevector(lo, hi, 0, 1, 2, 3, 4, 5, 6, 7);
          asm volatile("" : "+v"(af[i]));
        }
#pragma unroll
        for (int p = 0; p < 2; ++p)
#pragma unroll
          for (int j = 0; j < 4; ++j) {
            dp[p][2 * j] = __builtin_amdgcn_perm(dw[2 * p + 1][j], dw[2 * p][j], 0x05040100u);
            dp[p][2 * j + 1] = __builtin_amdgcn_perm(dw[2 * p + 1][j], dw[2 * p][j], 0x07060302u);
            asm volatile("" : "+v"(dp[p][2 * j]), "+v"(dp[p][2 * j + 1]));
          }
        asm volatile("" : "+v"(hbo), "+v"(mro));
      }
      if (best) X1_PUB();
      if (w == 7) {
        int spins = 0;
        KEY_LOAD();
        for (;;) {
          kmax = __builtin_amdgcn_readfirstlane(max64(umax(umax(kk[0], kk[1]), umax(kk[2], kk[3]))));
          if (kmax != SENT || ++spins > SPIN_CAP) break;
          __builtin_amdgcn_s_sleep(3);
          KEY_LOAD();
        }
        const int sel = 2047 - (int)(kmax & 2047u);
        u32x4 r0, r1;
        int sp2 = 0;
        for (;;) {
          r0 = ld128(rs, WS_BEST + (sel >> 3) * 2560 + l * 16);
          r1 = ld128(rs, WS_BEST + (sel >> 3) * 2560 + 1024 + i16 * 16);
          if (__all(max4(r0, max4(r1, 0u)) != SENT) || ++sp2 > SPIN_CAP) break;
          __builtin_amdgcn_s_sleep(1);
        }
        *(u32x4*)(lds + L_H2 + l * 16) = r0;
        *(u32x4*)(lds + L_H2 + 1024 + i16 * 16) = r1;
      }
      LBAR();
      {
        f32x4 ga = {0.f, 0.f, 0.f, 0.f};
#pragma unroll
        for (int i = 0; i < 5; ++i) ga = __builtin_amdgcn_mfma_f32_16x16x32_bf16(af[i], *(const bf16x8*)(lds + hbo + i * 256), ga, 0, 0, 0);
        *(f32x4*)(lds + mro) = ga;
        LBAR();
        if (w < 5) {
          const float* src = (const float*)(lds + L_MRED) + g4 * 4 + (l & 3);
          const float gg = rbf((src[0] + src[16]) + (src[32] + src[48]));
          const float uu = rbf((src[64] + src[80]) + (src[96] + src[112]));
          const float sg = rbf(gg * __builtin_amdgcn_rcpf(1.0f + __expf(-gg)));
          const float act = sg * uu;
          const unsigned ap = pack2(act, dppf<0xB1>(act));
          const unsigned a01 = dppu<0x00>(ap), a23 = dppu<0xAA>(ap);
          float y[8];
#pragma unroll
          for (int j = 0; j < 8; ++j) y[j] = dot2(dp[1][j], a23, dot2(dp[0][j], a01, 0.f));
          const float u0 = rs32(y[0], y[4]), u1 = rs32(y[1], y[5]), u2 = rs32(y[2], y[6]), u3 = rs32(y[3], y[7]);
          z0 = rs16(u0, u2); z1 = rs16(u1, u3);
        }
      }
    }
    ST64(((u32x2){nosent(__float_as_uint(z0)), nosent(__float_as_uint(z1))}),
         w < 5 ? WS_YP + (w * 16 + i16) * 3456 + fb * 32 + g4 * 8 : OOB);
    if (!best) OUT_ROW();
    if (b == 0 && t == 0) {
      const u64 ne = (((u64)epv.y << 32) | epv.x) + 1ull;
      ST64(((u32x2){(unsigned)ne, (unsigned)(ne >> 32)}), WS_EPOCH);
    }
    return;
  }
  if (!is_dn) return;
  if (w == 2) {
    unsigned km;
    int spins = 0;
    __builtin_amdgcn_s_sleep(KN);
    for (;;) {
      km = 0u;
#pragma unroll
      for (int i = 0; i < 4; ++i) km = umax(km, ld32(rs, WS_KEY + xcd * 4096 + (l + 64 * i) * 16));
      if (__all(km != SENT) || ++spins > SPIN_CAP) break;
      __builtin_amdgcn_s_sleep(6);
    }
    const unsigned kmax = __builtin_amdgcn_readfirstlane(max64(km));
    const int sel = 2047 - (int)(kmax & 2047u);
    u32x4 rv;
    int sp2 = 0;
    for (;;) {
      rv = ld128(rs, WS_BEST + (sel >> 3) * 2560 + ((l >> 1) & 1) * 1280 + dnb * 32 + (l & 1) * 16);
      if (__all(max4(rv, 0u) != SENT) || ++sp2 > SPIN_CAP) break;
      __builtin_amdgcn_s_sleep(1);
    }
    if (l < 4) *(u32x4*)(lds + L_H2 + l * 16) = rv;
    if (l == 0) *(unsigned*)(lds + L_MISC + 192) = kmax;
  }
  LBAR();
  if (w < 2) {
    const int cl = w * 8 + (l & 1) * 4 + g4, col = dnb * 16 + cl;
    const unsigned kmax = *(const unsigned*)(lds + L_MISC + 192);
    float V = 0.f;
    {
      const u32x4 sb4 = *(const u32x4*)(lds + L_P3 + 1280 + offA), wr4 = *(const u32x4*)(lds + L_P3 + 4 * 1280 + offA);
#pragma unroll
      for (int i = 0; i < 4; ++i) V = dot2(sb4[i], wr4[i], V);
      V = sum64(dot2(*(const unsigned*)(lds + L_P3 + 1280 + offB), *(const unsigned*)(lds + L_P3 + 4 * 1280 + offB), V));
    }
    const unsigned ord = (kmax & ~2047u) | 1024u;
    const float rsel = rbf(__uint_as_float((ord & 0x80000000u) ? (ord & 0x7fffffffu) : ~ord) + V);
    float h2 = bf2f(*(const u16*)(lds + L_H2 + cl * 2));
    float x1v = bf2f(*(const u16*)(lds + L_H2 + 32 + cl * 2));
    float gb = bf2f(*(const u16*)(lds + L_P3 + 3 * 1280 + col * 2));
    float sg = rbf(__builtin_amdgcn_rcpf(1.0f + __expf(-rsel)));
    int odo = i16 < 2 ? ((2047 - (int)(kmax & 2047u)) * DM + col) * 2 : OOB;
    int yo = WS_YP + (dnb * 2 + w) * 3456 + l * 16, yo3 = l < 24 ? yo + 3072 : OOB;
    asm volatile("" : "+v"(h2), "+v"(x1v), "+v"(gb), "+v"(sg), "+v"(odo));
    u32x4 yv[4];
    int spins = 0;
    __builtin_amdgcn_s_sleep(YD);
    for (;;) {
      asm volatile("" : "+v"(yo), "+v"(yo3));
      yv[0] = LD128S(yo, 0); yv[1] = LD128S(yo, 1024); yv[2] = LD128S(yo, 2048); yv[3] = LD128S(yo3, 0);
      if (__all(max4(yv[0], max4(yv[1], max4(yv[2], max4(yv[3], 0u)))) != SENT) || ++spins > SPIN_CAP) break;
      __builtin_amdgcn_s_sleep(YS);
    }
    const f32x4 ys = (__builtin_bit_cast(f32x4, yv[0]) + __builtin_bit_cast(f32x4, yv[1])) + (__builtin_bit_cast(f32x4, yv[2]) + __builtin_bit_cast(f32x4, yv[3]));
    float v = rs16(rs32(ys.x, ys.z), rs32(ys.y, ys.w));
    v += dppf<0x128>(v); v += dppf<0x124>(v); v += dppf<0x122>(v);
    const float y = rbf(rbf(v) * sg * 1.0345f);
    const float hh = rbf(h2 + y);
    __builtin_amdgcn_raw_buffer_store_b16((u16)(pack2(x1v + rbf(gb * hh), 0.f) & 0xffffu), ro, odo, 0, 16);
  }
}

extern "C" void kernel_launch(void* const* d_in, const int* in_sizes, int n_in, void* d_out, int out_size,
                              void* d_ws, size_t ws_size, hipStream_t stream) {
  k_all<<<256, 512, 0, stream>>>((const u16*)d_in[0], (const u16*)d_in[1], (const u16*)d_in[2], (const u16*)d_in[4],
                                 (const u16*)d_in[5], (const u16*)d_in[6], (const u16*)d_in[7], (const u16*)d_in[8],
                                 (const u16*)d_in[9], (char*)d_ws, (u16*)d_out);
}
```

```cpp
#include <hip/hip_runtime.h>
#include <stdint.h>

typedef __attribute__((ext_vector_type(8))) short bf16x8;
typedef __attribute__((ext_vector_type(4))) short s16x4;
typedef __attribute__((ext_vector_type(4))) float f32x4;
typedef __attribute__((ext_vector_type(2))) float f32x2;
typedef __attribute__((ext_vector_type(4))) unsigned u32x4;
typedef __attribute__((ext_vector_type(2))) unsigned u32x2;
typedef __attribute__((ext_vector_type(2))) __bf16 bf16x2;
typedef unsigned short u16;
typedef unsigned long long u64;
#define LAS __attribute__((address_space(3)))

#define GSL 2
#define HS 6
#define YD 16
#define KN 30
#define YS 2
#define XS 5
#define PGI 9
#define O1 5
#define H0 3
#define LS 8
#define TQ 3
#define ET 230
#define Q0 1
#define SP2 2
#define FL 0
#define RN 2
#define RD 1
#define WG 2
#define NF 1
#define W2 1
#define LA 8
#define GO 0
#define PBG 1
#define QE 1
#define XT 0
#define SPR 3
#define RCX 0
#define GT 230
#define LP 4
#define PFLIP 0
#define NS1 3
#define NK 14

#define S_LEN 2048
#define DM 640
#define FF 1728
#define SC1 16
#define SPIN_CAP (1 << 18)
#define SENT 0xffffffffu
#define OOB 0x40000000

__device__ __forceinline__ float bf2f(unsigned v) { return __uint_as_float(v << 16); }
__device__ __forceinline__ unsigned pack2v(f32x2 v) {
  bf16x2 r = __builtin_convertvector(v, bf16x2);
  return __builtin_bit_cast(unsigned, r);
}
__device__ __forceinline__ unsigned pack2(float a, float b) { return pack2v((f32x2){a, b}); }
__device__ __forceinline__ float rbf(float f) { return __uint_as_float(pack2(f, 0.f) << 16); }
__device__ __forceinline__ f32x2 unpk(unsigned u) {
  f32x2 r;
  r.x = __uint_as_float(u << 16);
  r.y = __uint_as_float(u & 0xffff0000u);
  return r;
}
__device__ __forceinline__ f32x2 rbf2(f32x2 v) { return unpk(pack2v(v)); }
__device__ __forceinline__ unsigned nosent(unsigned u) { return u < 0xfffffffeu ? u : 0xfffffffeu; }
__device__ __forceinline__ unsigned umax(unsigned a, unsigned b) { return a > b ? a : b; }
__device__ __forceinline__ unsigned max4(u32x4 v, unsigned m) { return umax(umax(umax(v.x, v.y), v.z), umax(v.w, m)); }
__device__ __forceinline__ float dot2(unsigned a, unsigned b, float c) {
  return __builtin_amdgcn_fdot2_f32_bf16(__builtin_bit_cast(bf16x2, a), __builtin_bit_cast(bf16x2, b), c, false);
}

template <int CTRL> __device__ __forceinline__ float dppf(float v) {
  return __builtin_bit_cast(float, __builtin_amdgcn_update_dpp(0, __builtin_bit_cast(int, v), CTRL, 0xF, 0xF, true));
}
template <int CTRL> __device__ __forceinline__ unsigned dppu(unsigned v) {
  return (unsigned)__builtin_amdgcn_update_dpp(0, (int)v, CTRL, 0xF, 0xF, true);
}
__device__ __forceinline__ float swapsum16(float v) {
  auto r = __builtin_amdgcn_permlane16_swap(__float_as_uint(v), __float_as_uint(v), false, false);
  return __uint_as_float(r[0]) + __uint_as_float(r[1]);
}
__device__ __forceinline__ float swapsum32(float v) {
  auto r = __builtin_amdgcn_permlane32_swap(__float_as_uint(v), __float_as_uint(v), false, false);
  return __uint_as_float(r[0]) + __uint_as_float(r[1]);
}
__device__ __forceinline__ float sum16(float v) {
  v += dppf<0xB1>(v); v += dppf<0x4E>(v); v += dppf<0x141>(v); v += dppf<0x140>(v);
  return v;
}
__device__ __forceinline__ float sum64(float v) {
  v += dppf<0xB1>(v); v += dppf<0x4E>(v); v += dppf<0x141>(v); v += dppf<0x140>(v);
  return swapsum32(swapsum16(v));
}
__device__ __forceinline__ unsigned max64(unsigned v) {
  v = umax(v, dppu<0xB1>(v)); v = umax(v, dppu<0x4E>(v)); v = umax(v, dppu<0x141>(v)); v = umax(v, dppu<0x140>(v));
  auto r = __builtin_amdgcn_permlane16_swap(v, v, false, false);
  v = umax(r[0], r[1]);
  auto q = __builtin_amdgcn_permlane32_swap(v, v, false, false);
  return umax(q[0], q[1]);
}
typedef __attribute__((ext_vector_type(4))) short v4i16_t;
__device__ __forceinline__ s16x4 trread(const char* p) {
  return __builtin_bit_cast(s16x4, __builtin_amdgcn_ds_read_tr16_b64_v4i16((LAS v4i16_t*)p));
}
__device__ __forceinline__ u32x4 ld128(__amdgpu_buffer_rsrc_t rs, int off) {
  asm volatile("" : "+v"(off));
  return __builtin_amdgcn_raw_buffer_load_b128(rs, off, 0, SC1);
}
__device__ __forceinline__ u32x2 ld64(__amdgpu_buffer_rsrc_t rs, int off) {
  asm volatile("" : "+v"(off));
  return __builtin_amdgcn_raw_buffer_load_b64(rs, off, 0, SC1);
}
__device__ __forceinline__ unsigned ld32(__amdgpu_buffer_rsrc_t rs, int off) {
  asm volatile("" : "+v"(off));
  return __builtin_amdgcn_raw_buffer_load_b32(rs, off, 0, SC1);
}
#define ST128(v, off) __builtin_amdgcn_raw_buffer_store_b128((v), rs, (off), 0, SC1)
#define ST64(v, off) __builtin_amdgcn_raw_buffer_store_b64((v), rs, (off), 0, SC1)
#define ST32(v, off) __builtin_amdgcn_raw_buffer_store_b32((v), rs, (off), 0, SC1)
#define SBAR() __builtin_amdgcn_sched_barrier(0)
#define ST128S(v, voff, soff) do { __builtin_amdgcn_raw_buffer_store_b128((v), rs, (voff), (soff), SC1); SBAR(); asm volatile("s_nop 1"); SBAR(); } while (0)
#define CFENCE() asm volatile("" ::: "memory")
#define LBAR() do { asm volatile("s_waitcnt lgkmcnt(0)" ::: "memory"); __builtin_amdgcn_s_barrier(); CFENCE(); } while (0)


#define LD128S(voff, soff) __builtin_amdgcn_raw_buffer_load_b128(rs, (voff), (soff), SC1)
#define DRAIN() asm volatile("s_waitcnt vmcnt(0)" ::: "memory")

#define WS_EPOCH 0
#define WS_FLAGE 217088
#define WS_FLAGA 16384
#define WS_FLAGO 20480
#define WS_ST 45056
#define WS_MODF 69632
#define WS_WT 262144
#define WS_WOT (WS_WT + 16 * 20 * 7680)
#define WS_UB (WS_WOT + 8 * 100 * 1024)
#define WS_FLAGL (WS_UB + 262144)
#define WS_FLAGF (WS_FLAGL + 32768)
#define WS_MODP (WS_FLAGL + 65536)
#define MPS 33792
#define WS_O (WS_MODP + 8 * MPS)
#define WS_HALO (WS_O + S_LEN * DM * 2)
#define WS_FILL (WS_HALO + 256 * 5120)
#define WS_A WS_FILL
#define WS_BEST (WS_A + S_LEN * DM * 2)
#define WS_KEY (WS_BEST + 256 * 2560)
#define WS_YP (WS_KEY + 8 * 4096)
#define FILL_PER 14336
#define WS_END (WS_FILL + 256 * FILL_PER)
static_assert(WS_YP + 40 * 6912 <= WS_END, "fill region too small");
static_assert(WS_FLAGE + 32768 <= WS_WT, "control area");

#define L_MISC 0
#define L_CNT 256
#define L_MRED 512
#define L_S 5120
#define L_DG 4096
#define L_OWN 7168
#define L_EA 8192
#define L_EB 10240
#define L_X 16384
#define L_STG (L_X + 7 * 16384)
#define KVS 248
#define L_KV (L_X - 16 * KVS)
#define L_XCH 10240
#define L_WO 53248
#define L_P3 155648
#define L_H2 L_S
#define L_U 7680
#define C_WL 24
#define C_ST 12
#define C_W2 25
#define C_WP 14
#define C_T1 28
#define C_P2 20
#define C_S2 22
#define C_E1 21
#define L_RST 64
#define C_PB 11
#define C_OW 10
#define C_EF 23
#define C_SD 26
#define C_C0 27
#define C_EX 9
#define C_HL 16
#define C_GO 19
#define C_CH 17
__device__ __forceinline__ unsigned opaque(unsigned v) { asm("" : "+s"(v)); return v; }
#define ONE_LO opaque(0x00003f80u)
#define ONE_HI opaque(0x3f800000u)

__device__ __forceinline__ unsigned xor_or(unsigned v, unsigned t, unsigned acc) { return __builtin_amdgcn_bitop3_b32(v, t, acc, 0xBE); }
__device__ __forceinline__ bool tag_ok(u32x4 g, unsigned tagl, unsigned tagh) {
  return ((((g.x ^ tagl) | (g.z ^ tagl)) & 0xffff0000u) | (g.y ^ tagh) | (g.w ^ tagh)) == 0u;
}
__device__ __forceinline__ float rs32(float a, float b) {
  auto r = __builtin_amdgcn_permlane32_swap(__float_as_uint(a), __float_as_uint(b), false, false);
  return __uint_as_float(r[0]) + __uint_as_float(r[1]);
}
__device__ __forceinline__ float rs16(float a, float b) {
  auto r = __builtin_amdgcn_permlane16_swap(__float_as_uint(a), __float_as_uint(b), false, false);
  return __uint_as_float(r[0]) + __uint_as_float(r[1]);
}
__device__ __forceinline__ float vmax(float a, float b) {
  float r;
  asm("v_max_f32 %0, %1, %2" : "=v"(r) : "v"(a), "v"(b));
  return r;
}
template <bool MAX> __device__ __forceinline__ void rows2(float& a, float& b) {
  auto r = __builtin_amdgcn_permlane16_swap(__float_as_uint(a), __float_as_uint(b), false, false);
  float t = MAX ? vmax(__uint_as_float(r[0]), __uint_as_float(r[1])) : __uint_as_float(r[0]) + __uint_as_float(r[1]);
  auto q = __builtin_amdgcn_permlane32_swap(__float_as_uint(t), __float_as_uint(t), false, false);
  t = MAX ? vmax(__uint_as_float(q[0]), __uint_as_float(q[1])) : __uint_as_float(q[0]) + __uint_as_float(q[1]);
  auto u = __builtin_amdgcn_permlane16_swap(__float_as_uint(t), __float_as_uint(t), false, false);
  a = __uint_as_float(u[0]); b = __uint_as_float(u[1]);
}
__device__ __forceinline__ unsigned padd(unsigned p, float al, float ah) { return pack2(dot2(p, ONE_LO, al), dot2(p, ONE_HI, ah)); }
__device__ __forceinline__ unsigned pmul(unsigned p, f32x2 m) { return pack2v(unpk(p) * m); }
__device__ __forceinline__ void wait_flags(__amdgpu_buffer_rsrc_t rs, int off, unsigned tagl, unsigned tagh) {
  int sp = 0;
  for (;;) {
    const u32x4 f = ld128(rs, off);
    if (__all(tag_ok(f, tagl, tagh)) || ++sp > SPIN_CAP) break;
    __builtin_amdgcn_s_sleep(2);
  }
  CFENCE();
}
__device__ __forceinline__ void wait_all256(__amdgpu_buffer_rsrc_t rs, int off, unsigned tagl, unsigned tagh) {
  int sp = 0;
  for (;;) {
    const u32x4 f0 = ld128(rs, off), f1 = ld128(rs, off + 1024);
    if (__all(tag_ok(f0, tagl, tagh) & tag_ok(f1, tagl, tagh)) || ++sp > SPIN_CAP) break;
    __builtin_amdgcn_s_sleep(8);
  }
  CFENCE();
}
__device__ __forceinline__ float silu(float cf) { return cf * __builtin_amdgcn_rcpf(1.0f + __expf(-cf)); }
__device__ __forceinline__ f32x4 mfma4(u32x2 a, u32x2 b, f32x4 c) {
  return __builtin_amdgcn_mfma_f32_4x4x4bf16_1k(__builtin_bit_cast(s16x4, a), __builtin_bit_cast(s16x4, b), c, 0, 0, 0);
}
template <int N> __device__ __forceinline__ u32x4 mod_partial(const u32x4 (&wm)[N], const u32x2 (&sd)[N], unsigned tag32) {
  f32x4 a0 = {0.f, 0.f, 0.f, 0.f}, a1 = a0;
#pragma unroll
  for (int i = 0; i < N; ++i) {
    a0 = mfma4(sd[i], (u32x2){wm[i].x, wm[i].y}, a0);
    a1 = mfma4(sd[i], (u32x2){wm[i].z, wm[i].w}, a1);
  }
  float v0 = rs32(a0.x, a1.x), v1 = rs32(a0.y, a1.y), v2 = rs32(a0.z, a1.z), v3 = rs32(a0.w, a1.w);
  v0 = rs16(v0, v2); v1 = rs16(v1, v3);
  v0 += dppf<0x128>(v0); v1 += dppf<0x128>(v1);
  return (u32x4){__float_as_uint(v0), tag32, __float_as_uint(v1), tag32};
}
template <int I> struct IC { static constexpr int v = I; };
template <int A, int B, class F> __device__ __forceinline__ void sfor(F&& f) {
  if constexpr (A + 1 == B) f(IC<A>{});
  else if constexpr (A < B) { sfor<A, (A + B) / 2>(f); sfor<(A + B) / 2, B>(f); }
}
template <int N> __device__ __forceinline__ void vmwait() { asm volatile("s_waitcnt vmcnt(%0)" ::"n"(N) : "memory"); SBAR(); }
#define MFMA(c, a, b) asm volatile("v_mfma_f32_16x16x32_bf16 %0, %1, %2, %0" : "+v"(c) : "v"(a), "v"(b))
#define MFMA0(c, a, b) asm volatile("v_mfma_f32_16x16x32_bf16 %0, %1, %2, 0" : "=&v"(c) : "v"(a), "v"(b))
#define MFMA4(d, a, b) asm volatile("v_mfma_f32_4x4x4_16b_bf16 %0, %1, %2, 0" : "=&v"(d) : "v"(a), "v"(b))
#define CVT(d, a, b) asm volatile("v_cvt_pk_bf16_f32 %0, %1, %2" : "=v"(d) : "v"(a), "v"(b))
#define MUL(d, a, b) asm volatile("v_mul_f32 %0, %1, %2" : "=v"(d) : "v"(a), "v"(b))
#define ADD(d, a, b) asm("v_add_f32 %0, %1, %2" : "=v"(d) : "v"(a), "v"(b))
#define LDX(d, voff, soff) asm volatile("buffer_load_dwordx4 %0, %1, %2, %3 offen sc1" : "=&v"(d) : "v"(voff), "s"(rs), "s"(soff) : "memory")
#define READY(d) asm volatile("" : "+v"(d))
#define LDXI(d, r, voff, soff) asm volatile("buffer_load_dwordx4 %0, %1, %2, %3 offen" : "=&v"(d) : "v"(voff), "s"(r), "s"(soff) : "memory")
#define LDXW(d, r, voff, soff) asm volatile("buffer_load_dword %0, %1, %2, %3 offen" : "=&v"(d) : "v"(voff), "s"(r), "s"(soff) : "memory")
#define LDXH(d, r, voff, soff) asm volatile("buffer_load_ushort %0, %1, %2, %3 offen" : "=&v"(d) : "v"(voff), "s"(r), "s"(soff) : "memory")
#define DM0(P) "s_mov_b32 m0, %4\n\ts_mov_b32 %0, %3\n\tbuffer_load_dwordx4 %1, %2, %0 offen " P "lds"
#define DM1(P) "\n\ts_add_u32 m0, m0, %6\n\ts_add_u32 %0, %0, %5\n\tbuffer_load_dwordx4 %1, %2, %0 offen " P "lds"
#define DM3(P) DM1(P) DM1(P) DM1(P)
#define DMAN(txt, r, voff, soff, ldsdst, gs, ls) do { \
    int t_; \
    asm volatile(txt : "=&s"(t_) : "v"(voff), "s"(r), "s"(soff), "s"(ldsdst), "n"(gs), "n"(ls) : "scc", "memory"); \
  } while (0)
__device__ __forceinline__ void wait_flag8(__amdgpu_buffer_rsrc_t rs, int off, u32x2 want) {
  unsigned n;
  u32x2 f;
  asm volatile("s_mov_b32 %1, %5\n"
               "1:\n\tbuffer_load_dwordx2 %0, %2, %3, 0 offen sc1\n\ts_waitcnt vmcnt(0)\n\tv_cmp_ne_u64 vcc, %4, %0\n\ts_cbranch_vccz 2f\n\t"
               "s_sleep 2\n\ts_sub_u32 %1, %1, 1\n\ts_cbranch_scc0 1b\n2:"
               : "=&v"(f), "=&s"(n) : "v"(off), "s"(rs), "s"(want), "n"(SPIN_CAP) : "vcc", "scc", "memory");
}
#define CODE_BYTES 34000
#define NE 12
#define NQ (21 - NK)
#define NFR (8 * NK + 8 * NQ)
#define FX (8 * NK + 2 * NQ - 2 - LA)
static_assert(FX >= 8 * NK && LA >= 8 && LA <= 12, "");
#define W2S 4
static_assert(LS == 8 && H0 >= 2 && H0 <= 4 && (NK == 14 || NK == 16), "");
constexpr int wissued(int s) {
  const int r = H0 + 2 * (s + 1) < s + 5 ? H0 + 2 * (s + 1) : s + 5;
  const int n = s < LS ? r : LS + 4 + RN * (s - LS + 1) / RD;
  return n > 20 ? 20 : n;
}
constexpr int epis(int h) { return h < 0 ? 0 : h >= 4 * NQ - 1 ? 28 : (h + 1) * 28 / (4 * NQ); }
constexpr int wlast() { int s = 0; while (wissued(s) < 20) ++s; return s; }
constexpr int phys(int c) { return (c == 0 || c == 9) ? 8 : c - 1; }
constexpr int fbase(int ks) { return ks < 2 ? 5 : ks < 10 ? 0 : ks < NK ? 4 : 5; }
constexpr int fks(int f) { return f < 8 * NK ? f >> 3 : NK + (f - 8 * NK) % (2 * NQ) / 2; }
constexpr int fm(int f) { return f < 8 * NK ? f & 7 : ((f - 8 * NK) / (2 * NQ) * 2 + (f & 1) + 7) & 7; }
constexpr int ckw(int s) { return s == LS ? C_WL : s == W2S ? C_W2 : ((s & 1) && s < NK) ? (s + 1) / 2 : -1; }
#define ALL4 0x01010101u
__device__ __forceinline__ void lds_set(char* lds, int idx, int byte) { *((volatile LAS unsigned char*)(lds + L_CNT) + idx * 4 + byte) = 1; }
__device__ __forceinline__ unsigned lds_cnt(char* lds, int idx) { return *((volatile LAS unsigned*)(lds + L_CNT) + idx); }
#define lds_wait(lds, idx, m) do { \
    unsigned v_, s_, n_; \
    asm volatile("s_mov_b32 %2, %6\n" \
                 "1:\n\tds_read_b32 %0, %3 offset:%4\n\ts_waitcnt lgkmcnt(0)\n\tv_readfirstlane_b32 %1, %0\n\ts_andn2_b32 %1, %5, %1\n\t" \
                 "s_cbranch_scc0 2f\n\ts_sleep 1\n\ts_sub_u32 %2, %2, 1\n\ts_cbranch_scc0 1b\n2:" \
                 : "=&v"(v_), "=&s"(s_), "=&s"(n_) : "v"((unsigned)(size_t)(LAS char*)(lds)), "n"(L_CNT + (idx) * 4), "s"(m), "n"(SPIN_CAP) : "scc", "memory"); \
  } while (0)
__device__ __forceinline__ unsigned row_stats2(const unsigned (&a)[5], const unsigned (&b)[5]) {
  float s0 = 0.f, q0 = 0.f, s1 = 0.f, q1 = 0.f;
#pragma unroll
  for (int i = 0; i < 5; ++i) {
    s0 = dot2(a[i], 0x3f803f80u, s0); q0 = dot2(a[i], a[i], q0);
    s1 = dot2(b[i], 0x3f803f80u, s1); q1 = dot2(b[i], b[i], q1);
  }
  const float v = sum16(rs16(rs32(s0, q0), rs32(s1, q1))) * (1.0f / DM);
  const auto r = __builtin_amdgcn_permlane32_swap(__float_as_uint(v), __float_as_uint(v), false, false);
  const float s = __uint_as_float(r[0]);
  float var = __uint_as_float(r[1]) - s * s;
  if (__builtin_expect(__any(s * s > 16.0f * var), 0)) {
    const float m0 = __uint_as_float(__builtin_amdgcn_readlane(r[0], 0)), m1 = __uint_as_float(__builtin_amdgcn_readlane(r[0], 16));
    q0 = 0.f; q1 = 0.f;
#pragma unroll
    for (int i = 0; i < 5; ++i) {
      const float d0 = dot2(a[i], ONE_LO, -m0), d1 = dot2(a[i], ONE_HI, -m0), e0 = dot2(b[i], ONE_LO, -m1), e1 = dot2(b[i], ONE_HI, -m1);
      q0 += d0 * d0; q0 += d1 * d1; q1 += e0 * e0; q1 += e1 * e1;
    }
    var = swapsum32(sum16(rs16(q0, q1))) * (1.0f / DM);
  }
  return pack2(-s, __builtin_amdgcn_sqrtf(rbf(rbf(var) + 1e-05f)));
}
__device__ __forceinline__ unsigned normalize(unsigned p, float mb, float isd) {
  return pack2v(rbf2(unpk(p) - (f32x2){mb, mb}) * (f32x2){isd, isd});
}

typedef __attribute__((ext_vector_type(16))) unsigned u32x16;
__global__ __launch_bounds__(512, 2) void k_all(const u16* a_x, const u16* a_wqkv, const u16* a_wo, const u16* a_wr, const u16* a_wg,
                                                const u16* a_wu, const u16* a_wd, const u16* a_c, const u16* a_wmod, char* a_ws, u16* a_out) {
  __shared__ __attribute__((aligned(256))) char lds[163840];
  u32x16 ka;
  u32x4 kb;
  u32x2 kc, tv;
  asm volatile("s_load_dwordx16 %0, %4, 0x0\n\ts_load_dwordx4 %1, %4, 0x40\n\ts_load_dwordx2 %2, %4, 0x50\n\ts_memrealtime %3"
               : "=&s"(ka), "=&s"(kb), "=&s"(kc), "=&s"(tv) : "s"(__builtin_amdgcn_kernarg_segment_ptr()));
  const auto code0 = __builtin_amdgcn_s_getpc();
  SBAR();
  int t = threadIdx.x, l = t & 63;
  const int w = __builtin_amdgcn_readfirstlane(t >> 6);
  const int b = blockIdx.x, xcd = b & 7, slot = b >> 3;
  int g4 = l >> 4, i16 = l & 15;
  const int mi = w ^ 4;

  const int Lc = slot + 32 * (w & 1);
  const bool urgent = Lc < 20, late = Lc >= 20 && Lc < 60;
  const int rb = (xcd >> 1) * 4 + (slot >> 3), hp = (xcd & 1) * 8 + (slot & 7);
  const int rg = ((xcd + 4) & 7) * 4 + (slot >> 3), cg = slot & 7;
  const int rc = ((xcd + RCX) & 7) * 32 + slot;
  const int hw = hp ^ PFLIP;
  const int Ggu = (slot >> 2) * 8 + xcd;
  const bool is_gu = slot < 16 && Ggu < 27;
  const int fb = Ggu * 4 + (slot & 3);
  const bool is_dn = b >= 128 && b < 168;
  const int dnb = b - 128;
  const int m2 = w & 3, kh = w >> 2;
  f32x4 mbias;
  int odst, odst4, kbo, vbo, qa[2][4];
  unsigned me;
#define ADDR_BLOCK() do { \
    const int lo = (rb == 0 && mi == 0) ? 8 : 0; \
    _Pragma("unroll") \
    for (int jj = 0; jj < 4; ++jj) { \
      const int kk = 4 * g4 + jj; \
      mbias[jj] = (kk >= lo && kk >= (i16 >> 1) + 1 && kk <= (i16 >> 1) + 8) ? 0.f : -1.0e30f; \
    } \
    const int mt = rb * 8 + mi, kc = 5 * hp + g4, kc4 = 5 * hp + 4; \
    odst = WS_O + (mt * 20 + (kc >> 2)) * 1024 + ((kc & 3) * 16 + i16) * 16; \
    odst4 = l < 16 ? WS_O + (mt * 20 + (kc4 >> 2)) * 1024 + ((kc4 & 3) * 16 + i16) * 16 : OOB; \
    kbo = L_KV + (16 * mi + 2 * i16) * KVS + g4 * 8; \
    vbo = L_KV + (16 * mi + 8 * g4 + 2 * ((l >> 2) & 3)) * KVS + 80 + (l & 3) * 8; \
      \
    const int qo = L_KV + (16 + 16 * mi + i16) * KVS + 160 + g4 * 8, zo = L_CNT + 128 + i16 * 8; \
    _Pragma("unroll") \
    for (int par = 0; par < 2; ++par) { \
      const bool pm = (i16 & 1) == par; \
      qa[par][0] = pm ? qo : zo; \
      qa[par][1] = (pm && g4 == 0) ? qo + 32 : zo; \
      qa[par][2] = (pm && g4 != 0) ? qo + 32 : zo; \
      qa[par][3] = (pm && g4 < 2) ? qo + 64 : zo; \
      asm volatile("" : "+v"(qa[par][0]), "+v"(qa[par][1]), "+v"(qa[par][2]), "+v"(qa[par][3])); \
    } \
    me = (i16 & 1) ? 0u : 0xffffffffu; \
    asm volatile("" : "+v"(odst), "+v"(odst4), "+v"(mbias), "+v"(kbo), "+v"(vbo), "+v"(me)); \
  } while (0)
#define TRQ(o) ({ const u32x2 lo_ = __builtin_bit_cast(u32x2, trread(lds + (o))), hi_ = __builtin_bit_cast(u32x2, trread(lds + (o) + 128)); (u32x4){lo_.x, lo_.y, hi_.x, hi_.y}; })
  const int bi = w < 4 ? w >> 1 : 2;
#define W_SRC() do { \
    const int pc = min(8 * (w & 1) + (l & 7), 14); \
    src = (l >> 3) * 3840 + pc * 16 + __builtin_amdgcn_sbfe(0xFE1F10, ((pc * 13) >> 3) & 24, 8) * 80 + ((rb + 16 * bi) * 8 * 3840 + hw * 80); \
    asm volatile("" : "+v"(src)); \
  } while (0)
  int src, mo, co;
  unsigned pfc;
  if (w < 4) {
    W_SRC();
  } else if (w >= 6) {
    const int cso = xcd * 160;
    mo = (l >> 3) * 15360 + (l & 7) * 16 + (cso * 3840 + Lc * 128); co = l * 4 + cso;
    asm volatile("" : "+v"(mo), "+v"(co));
  } else {
    const __amdgpu_buffer_rsrc_t rcode = __builtin_amdgcn_make_buffer_rsrc((void*)code0, 0, CODE_BYTES, 0x00020000);
    LDXW(pfc, rcode, t - 256 < 9 ? l * 128 : OOB, slot * 9 * 128);
#pragma unroll
    for (int i = 0; i < 4; ++i) *(u32x4*)(lds + (t - 256) * 16 + i * 2048) = (u32x4){0u, 0u, 0u, 0u};
  }
  SBAR();
  asm volatile("s_waitcnt lgkmcnt(0)\n\ts_barrier" : "+s"(ka), "+s"(kb), "+s"(kc), "+s"(tv) :: "memory");
  SBAR();
#define KPTR(v, i) ((void*)(((u64)(v)[(i) + 1] << 32) | (v)[i]))
  void *const x = KPTR(ka, 0), *const wqkv = KPTR(ka, 2), *const wo = KPTR(ka, 4), *const wr = KPTR(ka, 6), *const wg = KPTR(ka, 8);
  void *const wu = KPTR(ka, 10), *const wd = KPTR(ka, 12), *const cvec = KPTR(ka, 14), *const wmod = KPTR(kb, 0), *const out = KPTR(kc, 0);
  char* const ws = (char*)KPTR(kb, 2);
  const unsigned tstart = tv.x;
  const __amdgpu_buffer_rsrc_t rs = __builtin_amdgcn_make_buffer_rsrc(ws, 0, WS_END, 0x00020000);
  const __amdgpu_buffer_rsrc_t rm = __builtin_amdgcn_make_buffer_rsrc((void*)wmod, 0, DM * 7680, 0x00020000);
  u32x4 wm[10], rp[2], rq[3];
  unsigned cv;
  u32x2 epv;
#define MOD_LOAD(d) do { _Pragma("unroll") for (int i = 0; i < 10; ++i) LDXI(d[i], rm, mo, ((i & 1) + 16 * (i >> 1)) * 7680); } while (0)
  const __amdgpu_buffer_rsrc_t rw = __builtin_amdgcn_make_buffer_rsrc((void*)wqkv, 0, DM * 3840, 0x00020000);
  {
    if (w < 4) LDXI(rp[0], rw, src, 0);
    asm volatile("buffer_load_dwordx2 %0, off, %1, 0 sc1" : "=&v"(epv) : "s"(rs) : "memory");
    if (w >= 6) {
      const __amdgpu_buffer_rsrc_t rcv = __builtin_amdgcn_make_buffer_rsrc(cvec, 0, DM * 2, 0x00020000);
      LDXW(cv, rcv, co, 0);
      if (urgent) MOD_LOAD(wm);
    }
  }
  SBAR();
  int offA = l * 16, offB = 1024 + l * 4;
#define W_DST() \
  int wdst, sw = L_STG + (2 * bi + (w & 1)) * 1024 + ((l >> 1) & 3) * 256 + (l >> 3) * 32 + (l & 1) * 16, sr = L_STG + (w & 1) * 1024 + g4 * 256 + i16 * 8; \
  { \
    const int tt = 4 * (w & 1) + g4; \
    wdst = (WS_WT + hw * 153600 + (rb >> 2) * 7680 + tt * 1024 + (rb & 3) * (tt < 7 ? 256 : 128) + i16 * 16) | ((tt == 7 && i16 >= 8) ? OOB : 0); \
  }
  const __amdgpu_buffer_rsrc_t rx = __builtin_amdgcn_make_buffer_rsrc((void*)x, 0, S_LEN * DM * 2, 0x00020000);
  unsigned xs[5];
  unsigned tagl, tagh, tag32;
  u32x2 flagv;
#define TAGS() do { \
    READY(epv); \
    epv.x = __builtin_amdgcn_readfirstlane(epv.x); epv.y = __builtin_amdgcn_readfirstlane(epv.y); \
    const u64 tag = ((((u64)epv.y << 32) | epv.x) + 1ull) & 0xffffffffffffull; \
    tagl = (unsigned)(tag << 16); tagh = (unsigned)(tag >> 16); tag32 = (unsigned)tag; \
    flagv = (u32x2){tagl | 1u, tagh}; \
  } while (0)
#define GATHER(g, n, go, st) do { _Pragma("unroll") for (int q = 0; q < (n); ++q) LDX(g[q], go, q * (st)); } while (0)
#define GCHECK(g, n, go, st) do { \
    int spins = 0; \
    for (;;) { \
      _Pragma("unroll") for (int q = 0; q < (n); ++q) READY(g[q]); \
      unsigned b0 = g[0].y ^ tag32, b1 = g[0].w ^ tag32; \
      _Pragma("unroll") for (int q = 1; q < (n); ++q) { b0 = xor_or(g[q].y, tag32, b0); b1 = xor_or(g[q].w, tag32, b1); } \
      if (__builtin_expect(__all((b0 | b1) == 0u), 1)) break; \
      if (++spins > SPIN_CAP) break; \
      GATHER(g, n, go, st); \
      vmwait<0>(); \
    } \
  } while (0)
#define SUM4(g, e) ((__uint_as_float((g)[0].e) + __uint_as_float((g)[1].e)) + (__uint_as_float((g)[2].e) + __uint_as_float((g)[3].e)))
#define X_LOAD() do { LDXI(xl, rx, offA, (rc * 8 + w) * 1280); LDXW(xl4, rx, offB, (rc * 8 + w) * 1280); } while (0)
#define X_READY() do { READY(xl); READY(xl4); xs[0] = xl.x; xs[1] = xl.y; xs[2] = xl.z; xs[3] = xl.w; xs[4] = xl4; } while (0)

  const int wosrc = WS_WOT + cg * 102400;
  unsigned pfv[6];
  u32x4 hv[5];
  int hlo;
  if (w < 4) {
    u32x4 xv[2];
    unsigned xt[2];
    W_DST();
    sr += (w >> 1) * 2048; wdst += (w >> 1) * 4 * 7680;
    asm volatile("" : "+v"(sr), "+v"(wdst), "+v"(sw), "+v"(offA), "+v"(offB));
    const bool wok = rc < 200;
    const int ln = rc / 20, kc = (rc - ln * 20) * 4 + w, h5 = kc / 5;
    int src1 = (((l >> 3) & 3) + 20 * (l >> 5)) * 1280 + (l & 7) * 16 + (wok ? (h5 * 40 + (kc - h5 * 5) * 4) * 1280 + ln * 128 : OOB);
    int fe = FL ? (l < 16 ? WS_FLAGE + (hw * 16 + rb) * 128 + l * 8 : OOB) : (l == 0 ? WS_FLAGE + (hw * 16 + rb) * 64 : OOB);
    unsigned ecnt = l == 0 ? 1u : 0u, eca = (unsigned)(size_t)(LAS char*)lds + L_CNT + ((C_E1 + l) & 63) * 4;
    asm volatile("" : "+v"(src1), "+v"(fe), "+v"(ecnt), "+v"(eca));
    vmwait<0>();
    READY(rp[0]);
    *(u32x4*)(lds + sw) = rp[0];
    CFENCE();
    ST128(TRQ(sr), wdst);
    SBAR();
    lds_set(lds, C_ST, w);
    TAGS();
    lds_wait(lds, C_ST, ALL4);
#pragma unroll
    for (int r = 0; r < 2; ++r) { LDXI(xv[r], rx, offA, (rc * 8 + w + 4 * r) * 1280); LDXW(xt[r], rx, offB, (rc * 8 + w + 4 * r) * 1280); }
    {
      const __amdgpu_buffer_rsrc_t r1 = __builtin_amdgcn_make_buffer_rsrc((void*)wo, 0, DM * 1280, 0x00020000);
      LDXI(rp[1], r1, src1, 0);
    }
    ADDR_BLOCK();
    asm volatile("s_waitcnt vmcnt(5)\n\tds_add_rtn_u32 %0, %1, %0\n\ts_waitcnt lgkmcnt(0)" : "+v"(ecnt) : "v"(eca) : "memory");
    SBAR();
    __builtin_amdgcn_raw_buffer_store_b64(flagv, rs, fe, __builtin_amdgcn_readfirstlane(ecnt) == 3u ? 0 : OOB, SC1);
    SBAR();
    lds_set(lds, C_EF, w);
    int rdst = wok ? WS_WOT + (ln < 8 ? ln * 102400 + g4 * 1024 : (4 * (ln - 8) + g4) * 102400 + 4096) + (kc >> 2) * 5120 + (kc & 3) * 256 + i16 * 16 : OOB;
    asm volatile("" : "+v"(rdst));
    const int wsrc = WS_WT + hp * 153600 + w * 2048;
    int wo0 = l * 16, wo1 = w < 3 ? l * 16 + 1024 : (i16 < 8 ? 1024 + (g4 * 8 + i16) * 16 : OOB);
    int xb[2];
    xb[0] = L_X + 5 * 16384 + (i16 >> 3) * 1024 + (i16 & 7) * 128 + (g4 ^ (i16 >> 1)) * 16;
    xb[1] = xb[0] ^ 64;
    int dgo = L_DG + g4 * 64 + (l & 3) * 16, ebo = L_EB + i16 * 16;
    int ea0 = g4 == 0 ? L_EA + w * 512 + i16 * 16 : L_CNT + 128, ea1 = g4 == 0 ? ea0 + 256 : ea0;
    int kvo = L_KV + (16 + i16) * KVS + w * 64 + g4 * 8;
    int kvo1 = kvo + 32 - ((w == 3 && g4 == 3) ? 8 : 0);
    int hdo = w < 3 ? WS_HALO + (rb * 16 + hp) * 5120 + w * 2048 + l * 16 : OOB, hdo1 = w < 2 ? hdo + 1024 : OOB;
    asm volatile("" : "+v"(wo0), "+v"(wo1), "+v"(xb[0]), "+v"(xb[1]), "+v"(dgo), "+v"(ebo), "+v"(ea0), "+v"(ea1), "+v"(kvo), "+v"(kvo1),
                 "+v"(hdo), "+v"(hdo1));
    int feo = WS_FLAGE + (hp * 16 + i16) * (FL ? 128 : 64), rso = l < 32 ? L_RST + (w + g4 * 4) * 4 : L_RST + 32;
    int go = WS_MODP + (l >> 5) * MPS + (64 * w + 2 * (l & 31)) * 8;
    int gd = L_DG + w * 512 + ((l >> 2) & 7) * 64 + (l & 1) * 36 + (l & 2) * 4 + (l >> 5) * 18;
    asm volatile("" : "+v"(feo), "+v"(rso), "+v"(go), "+v"(gd));
    vmwait<1>();
    READY(rp[1]); READY(xv[0]); READY(xv[1]); READY(xt[0]); READY(xt[1]);
    *(u32x4*)(lds + sw + 10240) = rp[1];
    CFENCE();
    ST128(TRQ(sr + 10240), rdst);
    SBAR();
    lds_set(lds, C_S2, w);
    while ((unsigned)wall_clock64() - tstart < ET) __builtin_amdgcn_s_sleep(1);
    u32x4 g[4];
    u32x2 fe1;
    asm volatile("buffer_load_dwordx2 %0, %1, %2, 0 offen sc1" : "=&v"(fe1) : "v"(feo), "s"(rs) : "memory");
    if (GO == 0) GATHER(g, 4, go, 2 * MPS);
    {
      xs[0] = xv[0].x; xs[1] = xv[0].y; xs[2] = xv[0].z; xs[3] = xv[0].w; xs[4] = xt[0];
      const unsigned xb_[5] = {xv[1].x, xv[1].y, xv[1].z, xv[1].w, xt[1]};
      *(unsigned*)(lds + rso) = row_stats2(xs, xb_);
      CFENCE();
    }
    SBAR();
    if (GO == 1) GATHER(g, 4, go, 2 * MPS);
    vmwait<GO == 2 ? 0 : 4>();
    READY(fe1);
    if (__builtin_expect(!__all(fe1.x == flagv.x && fe1.y == flagv.y), 0)) wait_flag8(rs, feo, flagv);
    f32x4 acc[2][8], tmp[4];
    u32x4 wa[20], wb[20], dgv, ea[2];
#pragma unroll
    for (int s = 0; s < H0; ++s) {
      wa[s] = LD128S(wo0, wsrc + s * 7680); wb[s] = LD128S(wo1, wsrc + s * 7680); SBAR();
      if (GO == 2 && s == 0) GATHER(g, 4, go, 2 * MPS);
    }
    vmwait<GO == 2 ? 2 * H0 - 2 : 2 * H0>();
    GCHECK(g, 4, go, 2 * MPS);
    *(volatile LAS u16*)(lds + gd) = (u16)pack2(rbf(rs32(SUM4(g, x), SUM4(g, z))) + 1.0f, 0.f);
    lds_set(lds, w >> 1, 2 + (w & 1));
    lds_wait(lds, 0, ALL4);
    u32x4 xf[LA + 1];
    auto xrd = [&](auto F) __attribute__((always_inline)) {
      constexpr int f = decltype(F)::v;
      if constexpr (f < NFR) {
        constexpr int ks = fks(f), m = fm(f);
        if constexpr (ks < 20) xf[f % (LA + 1)] = *(const u32x4*)(lds + xb[ks & 1] + (phys(ks >> 1) - fbase(ks)) * 16384 + m * 2048);
        else xf[f % (LA + 1)] = *(const u32x4*)(lds + ebo + m * 256);
        if constexpr (f < 8 * NK && m == 7 && fbase(ks + 2) != fbase(ks)) {
          xb[ks & 1] += (fbase(ks + 2) - fbase(ks)) * 16384;
          asm volatile("" : "+v"(xb[ks & 1]));
        }
      }
    };
#define SCALE4(s) do { \
      MFMA4(tmp[2], ((u32x2){dgv.x, dgv.y}), ((u32x2){wb[s].x, wb[s].y})); \
      MFMA4(tmp[3], ((u32x2){dgv.z, dgv.w}), ((u32x2){wb[s].z, wb[s].w})); \
      MFMA4(tmp[0], ((u32x2){dgv.x, dgv.y}), ((u32x2){wa[s].x, wa[s].y})); \
      MFMA4(tmp[1], ((u32x2){dgv.z, dgv.w}), ((u32x2){wa[s].z, wa[s].w})); \
    } while (0)
#define SCVT(s, q) do { \
      if ((q) < 4) CVT(wa[s][(q) & 3], tmp[(q) >> 1][((q) & 1) * 2], tmp[(q) >> 1][((q) & 1) * 2 + 1]); \
      else CVT(wb[s][(q) & 3], tmp[(q) >> 1][((q) & 1) * 2], tmp[(q) >> 1][((q) & 1) * 2 + 1]); \
    } while (0)
    unsigned rdy, cone = 1u;
    LAS char *cset = (LAS char*)(lds + L_CNT) + w, *cget = (LAS char*)(lds + L_CNT);
    asm volatile("" : "+v"(cone), "+v"(cset), "+v"(cget));
#define CSET(idx) (*(volatile LAS unsigned char*)(cset + (idx) * 4) = (unsigned char)cone)
#define CGET(idx) (*(volatile LAS unsigned*)(cget + (idx) * 4))
#define CHECK_AT(m) do { \
      constexpr int sc = (s * 8 + (m) + LA) / 8 - 1; \
      if constexpr ((s * 8 + (m) + LA) % 8 == 0 && ckw(sc) >= 0) { \
        constexpr unsigned want = (sc == 1 || sc == W2S) ? ALL4 : 0x0101u; \
        if (__builtin_expect(__builtin_amdgcn_ballot_w64(rdy != want) != 0ull, 0)) { \
          lds_wait(lds, ckw(sc), want); \
        } \
        CFENCE(); \
      } \
    } while (0)
#define LGK(n) __builtin_amdgcn_s_waitcnt(0xC07F | ((n) << 8))
    dgv = *(const u32x4*)(lds + dgo);
    sfor<0, LA>(xrd);
    SBAR();
    SCALE4(0);
    SBAR();
    dgv = *(const u32x4*)(lds + dgo + 256);
    asm volatile("s_nop 5" ::: "memory");
    SBAR();
#pragma unroll
    for (int q = 0; q < 8; ++q) SCVT(0, q);
    asm volatile("s_nop 1" ::: "memory");
    SBAR();
    sfor<0, NK>([&](auto S) __attribute__((always_inline)) {
      constexpr int s = decltype(S)::v;
      sfor<0, 8>([&](auto M) __attribute__((always_inline)) {
        constexpr int m = decltype(M)::v, f = s * 8 + m;
        if (s == 0) MFMA0(acc[0][m], wa[s], xf[f % (LA + 1)]); else MFMA(acc[0][m], wa[s], xf[f % (LA + 1)]);
        SBAR();
        CHECK_AT(m);
        xrd(IC<f + LA>{});
        constexpr int n0 = wissued(s - 1), n1 = wissued(s);
        if constexpr (s + 1 < NE) {
          if (m == 1) SCALE4(s + 1);
          if (m == 2 && s + 2 < NE) dgv = *(const u32x4*)(lds + dgo + (s + 2) * 256);
          if (m == 3) SCVT(s + 1, 1);
          if (m == 5 || m == 6) SCVT(s + 1, 2 * m - 6);
        }
        if constexpr ((f + LA + 4) % 8 == 0 && ckw((f + LA + 4) / 8 - 1) >= 0) rdy = CGET(ckw((f + LA + 4) / 8 - 1));
        if (f == FX - 4) rdy = CGET(C_EX);
        if (m == 7) {
          if (s == 1) CSET(C_C0);
          if (s == LP) CSET(C_T1);
          if (s == 7) CSET(C_CH);
          if (s == wlast()) CSET(C_HL);
        }
        SBAR();
        if (s == 0) MFMA0(acc[1][m], wb[s], xf[f % (LA + 1)]); else MFMA(acc[1][m], wb[s], xf[f % (LA + 1)]);
        SBAR();
        if constexpr (s + 1 < NE) {
          if (m == 2) SCVT(s + 1, 0);
          if (m == 3) SCVT(s + 1, 2);
          if (m >= 4 && m <= 6) SCVT(s + 1, 2 * m - 5);
        }
        if constexpr (n1 > n0) {
          if (m == 0) wa[n0] = LD128S(wo0, wsrc + n0 * 7680);
          if (m == 1) wb[n0] = LD128S(wo1, wsrc + n0 * 7680);
        }
        if constexpr (n1 > n0 + 1) {
          if (m == 4) wa[n0 + 1] = LD128S(wo0, wsrc + (n0 + 1) * 7680);
          if (m == 6) wb[n0 + 1] = LD128S(wo1, wsrc + (n0 + 1) * 7680);
        }
        if (W2 && (m & 1) == 1) LGK(LA - 2);
        SBAR();
      });
      SBAR();
    });
    float isd[2][2], sc4[4];
    u32x2 pk;
    auto epi = [&](auto G, auto I) __attribute__((always_inline)) {
      constexpr int g = decltype(G)::v, i = decltype(I)::v, sub = i / 14, m = fm(8 * NK + g * 2 * NQ + sub), tl = i % 14 / 7, r = i % 7;
      if constexpr (r < 4) MUL(sc4[r], acc[tl][m][r], isd[g & 1][sub]);
      else if constexpr (r < 6) CVT(pk[r - 4], sc4[2 * r - 8], sc4[2 * r - 7]);
      else {
        *(u32x2*)(lds + (tl ? kvo1 : kvo) + m * 16 * KVS) = pk;
        if constexpr (m == 7) __builtin_amdgcn_raw_buffer_store_b128(((u32x4){pk.x, tag32, pk.y, tag32}), rs, tl ? hdo1 : hdo, 0, SC1);
      }
    };
    sfor<8 * NK, NFR>([&](auto F) __attribute__((always_inline)) {
      constexpr int f = decltype(F)::v, g = (f - 8 * NK) / (2 * NQ), q = (f - 8 * NK) % (2 * NQ), ks = fks(f), m = fm(f);
      MFMA(acc[0][m], ks < 20 ? wa[ks < 20 ? ks : 0] : ea[0], xf[f % (LA + 1)]);
      SBAR();
      if constexpr (ks == 20) { isd[g & 1][f & 1] = __uint_as_float(xf[f % (LA + 1)].w); }
      if constexpr (f == FX) {
        if (__builtin_expect(__builtin_amdgcn_ballot_w64(rdy != 0x0101u) != 0ull, 0)) { lds_wait(lds, C_EX, 0x0101u); }
        CFENCE();
      }
      xrd(IC<f + LA>{});
      if (f == FX - 4) rdy = CGET(C_EX);
      if constexpr (f == FX + 2) ea[0] = *(const u32x4*)(lds + ea0);
      if constexpr (f == FX + 3) ea[1] = *(const u32x4*)(lds + ea1);
      if constexpr (g > 0) sfor<epis(2 * q - 1), epis(2 * q)>([&](auto I) __attribute__((always_inline)) { epi(IC<g - 1>{}, I); });
      SBAR();
      MFMA(acc[1][m], ks < 20 ? wb[ks < 20 ? ks : 0] : ea[1], xf[f % (LA + 1)]);
      SBAR();
      if constexpr (g > 0) sfor<epis(2 * q), epis(2 * q + 1)>([&](auto I) __attribute__((always_inline)) { epi(IC<g - 1>{}, I); });
      if (f == 8 * NK + WG * NQ) CSET(C_P2);
      if (f == 8 * NK + HS * NQ) CSET(C_GO);
      if (W2 && (f & 1) == 1) LGK(LA - 2);
      SBAR();
    });
    asm volatile("s_nop 3" ::: "memory");
    SBAR();
#pragma unroll
    for (int i = 0; i < 4; ++i) {
      const int m = fm(NFR - 2) + (i >> 1);
      const f32x2 sc = {isd[1][i >> 1], isd[1][i >> 1]};
      *(u32x2*)(lds + ((i & 1) ? kvo1 : kvo) + m * 16 * KVS) = (u32x2){pack2v(acc[i & 1][m].xy * sc), pack2v(acc[i & 1][m].zw * sc)};
      SBAR();
    }
  } else {
    const int j = w & 1;
#define WO_FIRST() do { \
      if (NF) { lds_wait(lds, C_WP, 1u); lds_wait(lds, C_P2, ALL4); } \
      _Pragma("unroll") for (int i = 0; i < NF; ++i) \
        DMAN(DM0("sc1 ") DM3("sc1 ") DM3("sc1 ") DM3("sc1 "), rs, offA, wosrc + (w - 5) * 1024 + i * 30720, \
             (unsigned)(size_t)(LAS char*)lds + L_WO + (w - 5) * 1024 + i * 30720, 3072, 3072); \
    } while (0)
    if (w < 6) {
      int xo_ = (l >> 3) * 1280 + ((l & 7) ^ (4 * j + (l >> 4))) * 16;
      auto quad = [&](int c, int h) __attribute__((always_inline)) {
        DMAN(DM0("") DM3(""), rx, xo_, (rb * 128 + j * 8 + h * 64) * 1280 + c * 128,
             (unsigned)(size_t)(LAS char*)lds + L_X + j * 1024 + h * 8192 + ((c == 0 || c == 9) ? 8 : c - 1) * 16384, 16 * 1280, 2048);
      };
      asm volatile("" : "+v"(xo_));
#pragma unroll
      for (int q = 0; q < QE; ++q) quad(q >> 1, q & 1);
      ADDR_BLOCK();
      int flo = WS_FILL + b * FILL_PER + l * 16, fao = l == 0 ? WS_FLAGA + b * 8 : OOB;
      const int gq = g4 * 8 + xcd;
      int pfo = gq < 27 ? (slot * 20 + i16) * FF * 2 + gq * 128 : OOB, pfo1 = i16 < 4 ? pfo + 16 * FF * 2 : OOB;
      int pfo2 = gq < 27 ? (gq * 640 + slot * 20 + i16) * 128 : OOB, pfo3 = i16 < 4 ? pfo2 + 16 * 128 : OOB;
      hlo = rb > 0 ? WS_HALO + ((rb - 1) * 16 + hp) * 5120 + l * 16 : OOB;
      int spo = WS_ST + 4 + ((l < 32 ? cg : 8 + (cg >> 2)) * 20 + min(l & 31, 19)) * 64;
      u32x4 sent = {SENT, SENT, SENT, SENT};
      asm volatile("" : "+v"(pfo1), "+v"(pfo2), "+v"(pfo3), "+v"(flo), "+v"(fao), "+v"(hlo),
                   "+v"(pfo), "+v"(sent), "+v"(spo));
      if (XT) { while ((unsigned)wall_clock64() - tstart < XT) __builtin_amdgcn_s_sleep(1); } else lds_wait(lds, C_ST, ALL4);
      if (PBG && slot < 20) lds_wait(lds, C_PB, 1u);
#pragma unroll
      for (int q = QE; q < TQ; ++q) {
        if (q == Q0) lds_wait(lds, C_EF, ALL4);
        quad(q >> 1, q & 1);
      }
      static_assert(TQ >= 2 && TQ <= 8, "");
      sfor<TQ, (NK + TQ > 18 ? NK + TQ : 18)>([&](auto Q) {
        constexpr int q = decltype(Q)::v, n = q - TQ + 1;
        vmwait<4 * ((q < 18 ? q : 18) - n)>();
        if constexpr ((n & 1) == 0 && n <= NK) { lds_set(lds, n / 2 - 1, j); }
        if constexpr (q == 16) { lds_wait(lds, C_SD, 0x0101u); lds_wait(lds, C_S2, ALL4); }
        if constexpr (q < 18) quad(q >> 1, q & 1);
      });
      asm volatile("" ::"v"(pfc));
      TAGS();
      lds_wait(lds, C_C0, ALL4);
      quad(9, 0); quad(9, 1);
      if (j == 0) {
        lds_wait(lds, C_CH, 1u);
        int sp = 0;
        while (!__all(ld32(rs, spo) == tag32) && ++sp < SPIN_CAP) __builtin_amdgcn_s_sleep(8);
        CFENCE();
        lds_set(lds, C_WP, 0);
      }
      vmwait<0>();
      lds_set(lds, NK / 2, j);
      if (j == 1) {
        WO_FIRST();
      } else {
        lds_wait(lds, C_HL, ALL4);
#pragma unroll
        for (int i = 0; i < 13; ++i) __builtin_amdgcn_raw_buffer_store_b128(sent, rs, flo, i * 1024, SC1);
        ST128S(sent, flo, 13 * 1024);
        DRAIN();
        ST64(flagv, fao);
        lds_wait(lds, C_GO, 1u);
#pragma unroll
        for (int i = 0; i < 5; ++i) LDX(hv[i], hlo, i * 1024);
        const __amdgpu_buffer_rsrc_t rg0 = __builtin_amdgcn_make_buffer_rsrc((void*)wg, 0, DM * FF * 2, 0x00020000);
        const __amdgpu_buffer_rsrc_t rg1 = __builtin_amdgcn_make_buffer_rsrc((void*)wu, 0, DM * FF * 2, 0x00020000);
        const __amdgpu_buffer_rsrc_t rg2 = __builtin_amdgcn_make_buffer_rsrc((void*)wd, 0, DM * FF * 2, 0x00020000);
        LDXH(pfv[0], rg0, pfo, 0);
        LDXH(pfv[1], rg1, pfo, 0);
        LDXH(pfv[2], rg0, pfo1, 0);
        LDXH(pfv[3], rg1, pfo1, 0);
        LDXH(pfv[4], rg2, pfo2, 0);
        LDXH(pfv[5], rg2, pfo3, 0);
      }
    } else {
      __builtin_amdgcn_s_setprio(SPR);
      W_SRC();
#pragma unroll
      for (int i = 0; i < 3; ++i) LDXI(rq[i], rw, src, i * 128 * 3840);
      u32x2 dm = {(l & 2) ? 0u : 0xffffu << (16 * (l & 1)), (l & 2) ? 0xffffu << (16 * (l & 1)) : 0u};
      int bpo = (l >> 3) * 4, mpo;
      {
        const int kg = 8 * (Lc < 10 ? Lc : Lc - 10) + (l & 7);
        const int pg = Lc >= 20 ? kg + 128 : ((l & 8) ? kg : 48 + (kg & 15) * 10 + (kg >> 4) + (Lc < 10 ? 5 : 0));
        mpo = (WS_MODP + xcd * MPS + pg * 64 + (l >> 4) * 16) | (((l & 8) && (Lc < 10 || Lc >= 16)) ? OOB : 0);
      }
      W_DST();
      asm volatile("" : "+v"(dm), "+v"(bpo), "+v"(mpo), "+v"(sw), "+v"(sr), "+v"(wdst));
      u32x2 sd[10];
      {
        if (urgent) vmwait<13>(); else vmwait<3>();
        READY(cv);
        const unsigned s0 = pack2(silu(bf2f(cv)), silu(__uint_as_float(cv & 0xffff0000u)));
        unsigned v[5];
#pragma unroll
        for (int i = 0; i < 5; ++i) v[i] = __builtin_amdgcn_ds_bpermute(bpo + 32 * i, s0);
#pragma unroll
        for (int i = 0; i < 5; ++i) {
          const unsigned v0 = __builtin_amdgcn_perm(v[i], v[i], 0x01000100u), v1 = __builtin_amdgcn_perm(v[i], v[i], 0x03020302u);
          sd[2 * i] = (u32x2){v0 & dm.x, v0 & dm.y}; sd[2 * i + 1] = (u32x2){v1 & dm.x, v1 & dm.y};
          asm volatile("" : "+v"(sd[2 * i]), "+v"(sd[2 * i + 1]));
        }
      }
      TAGS();
      if (urgent) {
        vmwait<3>();
#pragma unroll
        for (int i = 0; i < 10; ++i) READY(wm[i]);
        ST128(mod_partial<10>(wm, sd, tag32), mpo);
        SBAR();
        lds_set(lds, C_PB, 0);
        vmwait<1>();
      } else {
        vmwait<0>();
      }
      u32x4 wv[5];
#pragma unroll
      for (int i = 0; i < 3; ++i) { READY(rq[i]); *(u32x4*)(lds + sw + i * 2048) = rq[i]; }
      CFENCE();
#pragma unroll
      for (int i = 2; i < 5; ++i) wv[i] = TRQ(sr + i * 2048);
      __builtin_amdgcn_raw_buffer_store_b128(wv[2], rs, wdst, 8 * 7680, SC1);
      SBAR();
      lds_wait(lds, C_ST, ALL4);
#pragma unroll
      for (int i = 0; i < 2; ++i) wv[i] = TRQ(sr + i * 2048);
      const int dp = (l & 1) * 36 + (l & 2) * 4 + (l >> 5) * 18, v2 = (l & 31) < 20 ? (l & 31) : 16 + (l & 3);
      int go1 = WS_MODP + (l >> 5) * MPS + (256 + 64 * j + 2 * (l & 31)) * 8, gd1 = L_DG + 2048 + j * 512 + ((l >> 2) & 7) * 64 + dp;
      int go2 = WS_MODP + (l >> 5) * MPS + (384 + rb * 80 + j * 40 + 2 * v2) * 8;
      int gd2 = j ? L_OWN + 320 + v2 * 4 + (l >> 5) * 2 : L_OWN + (v2 >> 2) * 64 + dp;
      const float one2 = j ? 0.0f : 1.0f;
      int oro = L_OWN + (l & 3) * 16;
      int sao = i16 == 4 * g4 + 1 ? L_OWN + 320 : L_CNT + 128;
      int ubo = WS_UB + (hw * 16 + rb) * 1024 + (j * 64 + l) * 8, fl = l == 0 ? WS_FLAGL + (hw * 32 + j * 16 + rb) * 64 : OOB;
      unsigned usel = i16 == 4 * g4 ? 0x3f803f80u : 0u;
      asm volatile("" : "+v"(go1), "+v"(gd1), "+v"(go2), "+v"(gd2), "+v"(oro), "+v"(sao), "+v"(ubo), "+v"(fl), "+v"(usel));
#pragma unroll
      for (int i = 0; i < 5; ++i) READY(wv[i]);
      lds_set(lds, C_SD, j);
      ADDR_BLOCK();
      vmwait<0>();
      ST64(flagv, fl + 32768);
      SBAR();
      while ((unsigned)wall_clock64() - tstart < GT) __builtin_amdgcn_s_sleep(1);
      u32x4 g1[4], g2[4];
      GATHER(g2, 4, go2, 2 * MPS);
      GATHER(g1, 4, go1, 2 * MPS);
      int uo = WS_UB + hp * 16384 + (l >> 5) * 1024 + j * 512 + (l & 31) * 16, so = WS_ST + (rb * 128 + j * 64 + l) * 8;
      int eao = L_EA + (j * 64 + (l & 31) * 2 + (l >> 5)) * 16, ebw = L_EB + (j * 64 + l) * 16;
      int flo = WS_FLAGL + (hp * 32 + j * 16 + i16) * 64;
      int f2o = flo + 32768, sto = (j == 1 && l < 8) ? WS_ST + rc * 64 + l * 8 : OOB, rso = L_RST + (l & 7) * 4;
      asm volatile("" : "+v"(sto), "+v"(rso), "+v"(uo), "+v"(so), "+v"(eao), "+v"(ebw), "+v"(flo), "+v"(f2o));
      vmwait<4>();
      GCHECK(g2, 4, go2, 2 * MPS);
      *(volatile LAS u16*)(lds + gd2) = (u16)pack2(rbf(rs32(SUM4(g2, x), SUM4(g2, z))) + one2, 0.f);
      lds_set(lds, C_OW, j);
      lds_wait(lds, C_OW, 0x0101u);
      {
        u32x4 dg[5], sp[5], wsc[5];
#pragma unroll
        for (int i = 4; i >= 0; --i) dg[i] = *(const u32x4*)(lds + oro + i * 64);
#pragma unroll
        for (int i = 0; i < 5; ++i) sp[i] = *(const u32x4*)(lds + sao + i * 16);
        const f32x4 z = {0.f, 0.f, 0.f, 0.f};
#pragma unroll
        for (int i = 4; i >= 0; --i) {
          const f32x4 t0 = mfma4((u32x2){dg[i].x, dg[i].y}, (u32x2){wv[i].x, wv[i].y}, z), t1 = mfma4((u32x2){dg[i].z, dg[i].w}, (u32x2){wv[i].z, wv[i].w}, z);
          wsc[i] = (u32x4){pack2v(t0.xy), pack2v(t0.zw), pack2v(t1.xy), pack2v(t1.zw)};
          if (i >= 3) __builtin_amdgcn_raw_buffer_store_b128(wsc[i], rs, wdst, i * 4 * 7680, SC1);
        }
        f32x4 ub = z;
#pragma unroll
        for (int i = 0; i < 5; ++i) ub = __builtin_amdgcn_mfma_f32_16x16x32_bf16(__builtin_bit_cast(bf16x8, sp[i]), __builtin_bit_cast(bf16x8, wv[i]), ub, 0, 0, 0);
#pragma unroll
        for (int i = 4; i >= 0; --i)
          ub = __builtin_amdgcn_mfma_f32_16x16x32_bf16(__builtin_bit_cast(bf16x8, ((u32x4){usel, usel, usel, usel})), __builtin_bit_cast(bf16x8, wsc[i]), ub, 0, 0, 0);
        ST64(((u32x2){__float_as_uint(ub.x), __float_as_uint(ub.y)}), ubo);
        SBAR();
        u32x2 f2;
        asm volatile("buffer_load_dwordx2 %0, %1, %2, 0 offen sc1" : "=&v"(f2) : "v"(f2o), "s"(rs) : "memory");
        vmwait<4>();
        GCHECK(g1, 4, go1, 2 * MPS);
        *(volatile LAS u16*)(lds + gd1) = (u16)pack2(rbf(rs32(SUM4(g1, x), SUM4(g1, z))) + 1.0f, 0.f);
        lds_set(lds, C_W2, j);
        vmwait<1>();
        ST64(flagv, fl);
        SBAR();
        vmwait<1>();
        READY(f2);
        if (__builtin_expect(!__all(f2.x == flagv.x && f2.y == flagv.y), 0)) wait_flag8(rs, f2o, flagv);
        lds_set(lds, C_W2, 2 + j);
        if (j == 1) {
          lds_wait(lds, 0, 0x01010000u);
          lds_wait(lds, 1, 0x01010000u);
          ST64(((u32x2){*(const unsigned*)(lds + rso), tag32}), sto);
        }
      }
      __builtin_amdgcn_s_setprio(SP2);
      {
        lds_wait(lds, C_T1, 1u);
        wait_flag8(rs, flo, flagv);
        lds_set(lds, C_WL, j);
        u32x4 ub[8];
        u32x2 sv = ld64(rs, so);
#pragma unroll
        for (int i = 0; i < 8; ++i) ub[i] = LD128S(uo, i * 2048);
        int sp = 0;
        while (!__all(sv.y == tag32) && ++sp < SPIN_CAP) { __builtin_amdgcn_s_sleep(2); sv = ld64(rs, so); }
        *(u32x4*)(lds + ebw) = (u32x4){__builtin_amdgcn_perm(sv.x, sv.x, 0x01000100u), sv.x, __builtin_amdgcn_perm(sv.x, sv.x, 0x03020302u),
                                       __float_as_uint(__builtin_amdgcn_rcpf(__uint_as_float(sv.x & 0xffff0000u))) & ~0x80u};
        float sm[4];
#pragma unroll
        for (int e = 0; e < 4; ++e) {
          float a0, a1, a2, a3;
          ADD(a0, ub[0][e], ub[1][e]); ADD(a1, ub[2][e], ub[3][e]); ADD(a2, ub[4][e], ub[5][e]); ADD(a3, ub[6][e], ub[7][e]);
          ADD(a0, a0, a1); ADD(a2, a2, a3); sm[e] = a0 + a2;
        }
        f32x2 v = {rs32(sm[0], sm[2]), rs32(sm[1], sm[3])};
        const unsigned p0 = pack2v(v);
        v -= unpk(p0);
        const unsigned p1 = pack2v(v);
        const unsigned p2 = pack2v(v - unpk(p1));
        *(u32x4*)(lds + eao) = (u32x4){__builtin_amdgcn_perm(p1, p0, 0x05040100u), __builtin_amdgcn_perm(p0, p2, 0x07060100u), __builtin_amdgcn_perm(p2, p1, 0x07060302u), 0u};
        CFENCE();
        lds_wait(lds, C_CH, ALL4);
        lds_set(lds, C_EX, j);
      }
      __builtin_amdgcn_s_setprio(0);
      u32x4 wl[10];
      if (late) {
        lds_wait(lds, C_HL, ALL4);
        MOD_LOAD(wl);
      }
      WO_FIRST();
      if (late) {
        vmwait<10 * NF>();
#pragma unroll
        for (int i = 0; i < 10; ++i) READY(wl[i]);
        ST128(mod_partial<10>(wl, sd, tag32), mpo);
      }
    }
  }
  asm volatile("" : "+v"(t));
  l = t & 63; g4 = l >> 4; i16 = l & 15;
  offA = l * 16; offB = 1024 + l * 4;
  LBAR();
  if (w == 4) {
    vmwait<6>();
#pragma unroll
    for (int i = 0; i < 5; ++i) READY(hv[i]);
    unsigned bad = 0u, bad1 = 0u;
#pragma unroll
    for (int i = 0; i < 5; ++i) { bad = xor_or(hv[i].y, tag32, bad); bad1 = xor_or(hv[i].w, tag32, bad1); }
    if (__builtin_expect(rb > 0 && !__all((bad | bad1) == 0u), 0)) {
      int sp = 0;
      do {
        __builtin_amdgcn_s_sleep(1);
#pragma unroll
        for (int i = 0; i < 5; ++i) LDX(hv[i], hlo, i * 1024);
        vmwait<0>();
        bad = 0u;
#pragma unroll
        for (int i = 0; i < 5; ++i) { READY(hv[i]); bad |= (hv[i].y ^ tag32) | (hv[i].w ^ tag32); }
      } while (!__all(bad == 0u) && ++sp < SPIN_CAP);
    }
#pragma unroll
    for (int i = 0; i < 5; ++i) *(u32x2*)(lds + L_KV + i16 * KVS + g4 * 8 + i * 32) = (u32x2){hv[i].x, hv[i].z};
    CFENCE();
  }
  u32x4 mw[5], dw[4];
  const __amdgpu_buffer_rsrc_t rgu = __builtin_amdgcn_make_buffer_rsrc((void*)(kh ? wu : wg), 0, DM * FF * 2, 0x00020000);
  const int guo = is_gu ? ((t & 255) >> 1) * (FF * 2) + fb * 32 + (t & 1) * 16 : OOB;
  SBAR();

  u32x4 xl;
  unsigned xl4;
  {
    const char* kb = lds + kbo;
    f32x4 sc[2] = {mbias, mbias};
    bf16x8 vf[2][2];
    {
      typedef __attribute__((ext_vector_type(6))) unsigned u32x6;
      u32x6 kk[2];
      u32x4 qb[2][2];
#pragma unroll
      for (int par = 0; par < 2; ++par) {
        const u32x2 k0 = *(const u32x2*)(kb + par * KVS), k1 = *(const u32x2*)(kb + par * KVS + 32), k2 = *(const u32x2*)(kb + par * KVS + 64);
        kk[par] = (u32x6){k0.x, k0.y, k1.x, k1.y, k2.x, k2.y};
#pragma unroll
        for (int h = 0; h < 2; ++h) {
          const u32x2 a = *(const u32x2*)(lds + qa[par][2 * h]), c = *(const u32x2*)(lds + qa[par][2 * h + 1]);
          qb[h][par] = (u32x4){a.x, a.y, c.x, c.y};
        }
      }
      const char* vb = lds + vbo;
#pragma unroll
      for (int h = 0; h < 2; ++h)
#pragma unroll
        for (int d = 0; d < 2; ++d) {
          const s16x4 lo = trread(vb + h * 40 + d * 32), hi = trread(vb + KVS + h * 40 + d * 32);
          vf[h][d] = __builtin_shufflevector(lo, hi, 0, 1, 2, 3, 4, 5, 6, 7);
        }
      SBAR();
      asm volatile("" : "+v"(kk[0]), "+v"(kk[1]));
#pragma unroll
      for (int par = 0; par < 2; ++par) {
        sc[0] = __builtin_amdgcn_mfma_f32_16x16x32_bf16(__builtin_bit_cast(bf16x8, __builtin_shufflevector(kk[par], kk[par], 0, 1, 2, 3)),
                                                        __builtin_bit_cast(bf16x8, qb[0][par]), sc[0], 0, 0, 0);
        sc[1] = __builtin_amdgcn_mfma_f32_16x16x32_bf16(__builtin_bit_cast(bf16x8, __builtin_shufflevector(kk[par], kk[par], 2, 3, 4, 5)),
                                                        __builtin_bit_cast(bf16x8, qb[1][par]), sc[1], 0, 0, 0);
      }
    }
    f32x4 od[2][2], lg[2];
    float mx[2], sm[2];
#pragma unroll
    for (int h = 0; h < 2; ++h) {
      const f32x2 sc2 = {0.22360679774997896f, 0.22360679774997896f};
      const f32x2 a = rbf2(rbf2(sc[h].xy) * sc2), c = rbf2(rbf2(sc[h].zw) * sc2);
      lg[h] = (f32x4){a.x, a.y, c.x, c.y};
      mx[h] = fmaxf(fmaxf(a.x, a.y), fmaxf(c.x, c.y));
    }
    rows2<true>(mx[0], mx[1]);
#pragma unroll
    for (int h = 0; h < 2; ++h) {
      const float k2 = 1.4426950408889634f;
      const float nm = mx[h] * -k2;
#pragma unroll
      for (int j = 0; j < 4; ++j) lg[h][j] = __builtin_amdgcn_exp2f(__builtin_fmaf(lg[h][j], k2, nm));
      sm[h] = (lg[h][0] + lg[h][1]) + (lg[h][2] + lg[h][3]);
    }
    rows2<false>(sm[0], sm[1]);
#pragma unroll
    for (int h = 0; h < 2; ++h) {
      const float inv = __builtin_amdgcn_rcpf(sm[h]);
      const unsigned p01 = pack2(lg[h][0] * inv, lg[h][1] * inv), p23 = pack2(lg[h][2] * inv, lg[h][3] * inv);
      const bf16x8 pf = __builtin_bit_cast(bf16x8, ((u32x4){p01 & me, p23 & me, p01 & ~me, p23 & ~me}));
#pragma unroll
      for (int d = 0; d < 2; ++d)
        od[h][d] = __builtin_amdgcn_mfma_f32_16x16x32_bf16(vf[h][d], pf, (f32x4){0.f, 0.f, 0.f, 0.f}, 0, 0, 0);
    }
    ST128(((u32x4){pack2v(od[0][0].xy), pack2v(od[0][0].zw), pack2v(od[1][0].xy), pack2v(od[1][0].zw)}), odst);
    ST128(((u32x4){pack2v(od[0][1].xy), pack2v(od[0][1].zw), pack2v(od[1][1].xy), pack2v(od[1][1].zw)}), odst4);
    SBAR();
#define WO_REST() do { \
      _Pragma("unroll") for (int i = 0; i < 2 - NF; ++i) \
        DMAN(DM0("sc1 ") DM3("sc1 "), rs, offA, wosrc + (30 * NF + 32 * i + w) * 1024, (unsigned)(size_t)(LAS char*)lds + L_WO + (30 * NF + 32 * i + w) * 1024, 8192, 8192); \
      DMAN(DM0("sc1 ") DM3("sc1 ") DM1("sc1 "), rs, offA, wosrc + (64 - 2 * NF + w) * 1024, (unsigned)(size_t)(LAS char*)lds + L_WO + (64 - 2 * NF + w) * 1024, 8192, 8192); \
      if (w >= 4) X_LOAD(); \
    } while (0)
    int fo = l == 0 ? WS_FLAGO + (rb * 8 + mi) * 128 + hp * 8 : OOB;
    asm volatile("" : "+v"(fo));
    DRAIN();
    asm volatile("" ::"v"(pfv[0]), "v"(pfv[1]), "v"(pfv[2]), "v"(pfv[3]), "v"(pfv[4]), "v"(pfv[5]));
    ST64(flagv, fo);
    SBAR();
    WO_REST();
  }
  if (w == 3 && b >= 216) {
    const int Lr = b - 196;
    u32x2 g[8];
    int sp = 0;
    for (;;) {
      unsigned bad = 0u;
#pragma unroll
      for (int q = 0; q < 8; ++q) g[q] = ld64(rs, WS_MODP + q * MPS + (Lr * 64 + 384 + l) * 8);
#pragma unroll
      for (int q = 0; q < 8; ++q) bad = xor_or(g[q].y, tag32, bad);
      if (__all(bad == 0u) || ++sp > SPIN_CAP) break;
      __builtin_amdgcn_s_sleep(4);
    }
    float s = rbf(SUM4(g, x) + SUM4(g + 4, x));
    if (Lr >= 40 && Lr < 50) s = rbf(1.0f + s);
    ST64(((u32x2){tagl | (__float_as_uint(s) >> 16), tagh}), WS_MODF + ((Lr - 20) * 64 + l) * 8);
  }
  int wqo = L_WO + kh * 50 * 1024 + l * 16, ofo = WS_FLAGO + (rg * 4 + m2) * 128 + kh * 64 + (l & 7) * 8;
  int osrc = WS_O + ((rg * 4 + m2) * 20 + kh * 10) * 1024;
  asm volatile("" : "+v"(wqo), "+v"(ofo), "+s"(osrc));
  vmwait<0>();
  if (w >= 4) X_READY();
  LBAR();

  f32x2 xf[5];
  {
    const char* wb = lds + wqo;
    bf16x8 wq[25];
    wait_flag8(rs, ofo, flagv);
    u32x4 of[10], pg[5];
    unsigned wrp[2];
#pragma unroll
    for (int i = 0; i < O1; ++i) { of[i] = LD128S(offA, osrc + i * 1024); SBAR(); }
    SBAR();
#pragma unroll
    for (int i = 0; i < 25; ++i) wq[i] = *(const bf16x8*)(wb + i * 1024);
    int xwo = L_XCH + (kh * 64 + m2 * 16 + i16) * 336 + g4 * 16;
    int xr0 = L_XCH + (t >> 3) * 336 + (t & 7) * 32, xr1 = L_XCH + (t >> 3) * 336 + 256 + (t & 7) * 8;
    int ad0 = WS_A + (rg * 64 + (t >> 3)) * 1024 + cg * 128 + (t & 7) * 16, ad1 = WS_A + S_LEN * 1024 + (cg * S_LEN + rg * 64) * 32 + t * 4;
    int pgo = w < 4 ? WS_MODF + t * 16 : OOB, pgo4 = w < 4 ? WS_MODF + (w * 320 + 256 + l) * 16 : OOB;
    asm volatile("" : "+v"(xwo), "+v"(xr0), "+v"(xr1), "+v"(ad0), "+v"(ad1), "+v"(pgo), "+v"(pgo4));
    SBAR();
#pragma unroll
    for (int i = O1; i < 10; ++i) { of[i] = LD128S(offA, osrc + i * 1024); SBAR(); }
    SBAR();
    u32x4 fa0, fa1;
    if (w == 3) { fa0 = ld128(rs, WS_FLAGA + l * 16); fa1 = ld128(rs, WS_FLAGA + 1024 + l * 16); }
    SBAR();
    f32x4 a2[5];
#pragma unroll
    for (int i = 0; i < 10; ++i) {
      const bf16x8 ofr = __builtin_bit_cast(bf16x8, of[i]);
#pragma unroll
      for (int n = 0; n < 5; ++n) {
        const int f = i * 5 + n;
        if (i == 0) MFMA0(a2[n], wq[f % 25], ofr); else MFMA(a2[n], wq[f % 25], ofr);
        SBAR();
        if (f < 25) wq[f] = *(const bf16x8*)(wb + (25 + f) * 1024);
        SBAR();
      }
      if (i == PGI) {
#pragma unroll
        for (int j = 0; j < 4; ++j) pg[j] = LD128S(pgo, j * 5120);
        pg[4] = LD128S(pgo4, 0);
        const __amdgpu_buffer_rsrc_t rwr = __builtin_amdgcn_make_buffer_rsrc((void*)wr, 0, w < 4 ? DM * 2 : 0, 0x00020000);
        wrp[0] = __builtin_amdgcn_raw_buffer_load_b32(rwr, t * 4, 0, 0);
        wrp[1] = __builtin_amdgcn_raw_buffer_load_b32(rwr, offB, 0, 0);
      }
      if (i > XS && i <= XS + NS1) mw[i - XS - 1] = __builtin_amdgcn_raw_buffer_load_b128(rgu, guo, (i - XS - 1) * 128 * FF * 2, 0);
      SBAR();
    }
    asm volatile("s_nop 7\n\ts_nop 1" ::: "memory");
    SBAR();
#pragma unroll
    for (int n = 0; n < 5; ++n) *(f32x4*)(lds + xwo + n * 64) = a2[n];
    if (w == 3 && !__all(tag_ok(fa0, tagl, tagh) & tag_ok(fa1, tagl, tagh))) wait_all256(rs, WS_FLAGA + l * 16, tagl, tagh);
    LBAR();
    {
      const f32x4 s0 = *(const f32x4*)(lds + xr0) + *(const f32x4*)(lds + xr0 + 64 * 336);
      const f32x4 s1 = *(const f32x4*)(lds + xr0 + 16) + *(const f32x4*)(lds + xr0 + 64 * 336 + 16);
      const f32x2 s2 = *(const f32x2*)(lds + xr1) + *(const f32x2*)(lds + xr1 + 64 * 336);
      ST128(((u32x4){nosent(pack2v(s0.xy)), nosent(pack2v(s0.zw)), nosent(pack2v(s1.xy)), nosent(pack2v(s1.zw))}), ad0);
      ST32(nosent(pack2v(s2)), ad1);
    }
    if (w < 4) {
#define PG_OK() __all(tag_ok(pg[0], tagl, tagh) & tag_ok(pg[1], tagl, tagh) & tag_ok(pg[2], tagl, tagh) & tag_ok(pg[3], tagl, tagh) & tag_ok(pg[4], tagl, tagh))
      if (__builtin_expect(!PG_OK(), 0)) {
        int spins = 0;
        do {
          __builtin_amdgcn_s_sleep(4);
#pragma unroll
          for (int j = 0; j < 4; ++j) pg[j] = ld128(rs, pgo + j * 5120);
          pg[4] = ld128(rs, pgo4);
        } while (!PG_OK() && ++spins < SPIN_CAP);
      }
      unsigned pp[5];
#pragma unroll
      for (int j = 0; j < 5; ++j) pp[j] = (pg[j].x & 0xffffu) | (pg[j].z << 16);
#pragma unroll
      for (int j = 0; j < 4; ++j) *(unsigned*)(lds + L_P3 + j * 1280 + t * 4) = pp[j];
      *(unsigned*)(lds + L_P3 + w * 1280 + offB) = pp[4];
      *(unsigned*)(lds + L_P3 + 4 * 1280 + t * 4) = wrp[0];
      {
        const f32x2 u = unpk(pp[2]) * unpk(wrp[0]);
        const unsigned uh = pack2v(u);
        *(unsigned*)(lds + L_U + t * 4) = uh;
        *(unsigned*)(lds + L_U + 1280 + t * 4) = pack2v(u - unpk(uh));
      }
      if (w == 2) {
        const f32x2 u = unpk(pp[4]) * unpk(wrp[1]);
        const unsigned uh = pack2v(u);
        *(unsigned*)(lds + L_U + offB) = uh;
        *(unsigned*)(lds + L_U + 1280 + offB) = pack2v(u - unpk(uh));
        *(unsigned*)(lds + L_P3 + 4 * 1280 + offB) = wrp[1];
      }
    }
    LBAR();
  }

  const int row3 = rc * 8 + w;
  const __amdgpu_buffer_rsrc_t ro = __builtin_amdgcn_make_buffer_rsrc((void*)out, 0, S_LEN * DM * 2, 0x00020000);
  unsigned x1[5], hq[5];
  f32x2 gbf[5], sbf[5], cbf[5];
  float mb, isd;
  unsigned key;
  int kdst;
  {
    const int asrc = WS_A + row3 * 1024 + l * 16, asrc2 = WS_A + S_LEN * 1024 + ((l >> 3) * S_LEN + row3) * 32 + (l & 7) * 4;
    u32x4 av = ld128(rs, asrc);
    unsigned av2 = ld32(rs, asrc2);
    SBAR();
    unsigned pr[5][5];
#pragma unroll
    for (int a = 0; a < 5; ++a) {
      const u32x4 v = *(const u32x4*)(lds + L_P3 + a * 1280 + offA);
      pr[a][0] = v.x; pr[a][1] = v.y; pr[a][2] = v.z; pr[a][3] = v.w;
      pr[a][4] = *(const unsigned*)(lds + L_P3 + a * 1280 + offB);
    }
    kdst = l < 8 ? WS_KEY + l * 4096 + rc * 16 : OOB;
    asm volatile("" : "+v"(kdst));
    unsigned gal[5], gah[5], uh[5], ul[5];
    {
      const u32x4 v = *(const u32x4*)(lds + L_U + offA), v2 = *(const u32x4*)(lds + L_U + 1280 + offA);
      uh[0] = v.x; uh[1] = v.y; uh[2] = v.z; uh[3] = v.w; uh[4] = *(const unsigned*)(lds + L_U + offB);
      ul[0] = v2.x; ul[1] = v2.y; ul[2] = v2.z; ul[3] = v2.w; ul[4] = *(const unsigned*)(lds + L_U + 1280 + offB);
    }
    float U = 0.f;
#pragma unroll
    for (int i = 0; i < 5; ++i) {
      gal[i] = pr[0][i] & 0xffffu; gah[i] = pr[0][i] & 0xffff0000u;
      U = dot2(pr[2][i], pr[4][i], U);
      asm volatile("" : "+v"(gal[i]), "+v"(gah[i]), "+v"(uh[i]), "+v"(ul[i]));
    }
    asm volatile("" : "+v"(U));
#pragma unroll
    for (int i = 0; i < 5; ++i) { xf[i] = unpk(xs[i]); asm volatile("" : "+v"(xf[i])); }
#pragma unroll
    for (int i = 0; i < 5; ++i) {
      sbf[i] = unpk(pr[1][i]); cbf[i] = unpk(pr[2][i]); gbf[i] = unpk(pr[3][i]);
      asm volatile("" : "+v"(sbf[i]), "+v"(cbf[i]), "+v"(gbf[i]));
    }
    int spins = 0;
    for (;;) {
      if (__all(max4(av, av2) != SENT) || ++spins > SPIN_CAP) break;
      __builtin_amdgcn_s_sleep(2);
      av = ld128(rs, asrc);
      av2 = ld32(rs, asrc2);
    }
    const unsigned aa[5] = {av.x, av.y, av.z, av.w, av2};
    float s = 0.f, q = 0.f, d0 = 0.f, d1 = 0.f;
#pragma unroll
    for (int i = 0; i < 5; ++i) {
      x1[i] = padd(pack2(dot2(aa[i], gal[i], 0.f), dot2(aa[i], gah[i], 0.f)), xf[i].x, xf[i].y);
      s = dot2(x1[i], 0x3f803f80u, s); q = dot2(x1[i], x1[i], q);
      d0 = dot2(x1[i], uh[i], d0); d1 = dot2(x1[i], ul[i], d1);
    }
    {
      float v = rs16(rs32(s, q), rs32(d0 + d1, U));
      v += dppf<0xB1>(v); v += dppf<0x4E>(v); v += dppf<0x141>(v); v += dppf<0x140>(v);
      s = __uint_as_float(__builtin_amdgcn_readlane(__float_as_uint(v), 0)) * (1.0f / DM);
      q = __uint_as_float(__builtin_amdgcn_readlane(__float_as_uint(v), 32)) * (1.0f / DM);
      d0 = __uint_as_float(__builtin_amdgcn_readlane(__float_as_uint(v), 16));
      U = __uint_as_float(__builtin_amdgcn_readlane(__float_as_uint(v), 48));
    }
    float var = q - s * s;
    if (__builtin_expect(__builtin_amdgcn_readfirstlane(s * s > 16.0f * var ? 1 : 0), 0)) {
      float q2 = 0.f;
#pragma unroll
      for (int i = 0; i < 5; ++i) {
        const float e0 = dot2(x1[i], ONE_LO, -s), e1 = dot2(x1[i], ONE_HI, -s);
        q2 += e0 * e0; q2 += e1 * e1;
      }
      var = sum64(q2) * (1.0f / DM);
    }
    mb = rbf(s);
    isd = __builtin_amdgcn_rcpf(rbf(__builtin_amdgcn_sqrtf(rbf(rbf(var) + 1e-05f))));
    {
      const float r = (d0 - mb * U) * isd;
      const unsigned u = __float_as_uint(r);
      unsigned ord = u ^ ((unsigned)((int)u >> 31) | 0x80000000u);
      if (r != r) ord = 0u;
      key = (ord & ~2047u) | (unsigned)(2047 - row3);
    }
    *(unsigned*)(lds + L_MISC + 128 + w * 4) = key;
  }
  LBAR();
  bool best;
  {
    const u32x4 ka = *(const u32x4*)(lds + L_MISC + 128), kb = *(const u32x4*)(lds + L_MISC + 144);
    best = __builtin_amdgcn_readfirstlane(max4(ka, max4(kb, 0u)) == key ? 1 : 0) != 0;
  }
  ST32(key, best ? kdst : OOB);
  SBAR();
#define S2_LOADS() do { \
    _Pragma("unroll") for (int i = NS1; i < 5; ++i) mw[i] = __builtin_amdgcn_raw_buffer_load_b128(rgu, guo, i * 128 * FF * 2, 0); \
    SBAR(); \
  } while (0)
#define HQ() do { _Pragma("unroll") for (int i = 0; i < 5; ++i) hq[i] = padd(pmul(normalize(x1[i], mb, isd), cbf[i]), sbf[i].x, sbf[i].y); } while (0)
  if (best) {
    __builtin_amdgcn_s_setprio(2);
    HQ();
    ST128(((u32x4){nosent(hq[0]), nosent(hq[1]), nosent(hq[2]), nosent(hq[3])}), WS_BEST + rc * 2560 + offA);
    ST32(nosent(hq[4]), WS_BEST + rc * 2560 + offB);
    __builtin_amdgcn_s_setprio(0);
    SBAR();
    S2_LOADS();
  } else {
    __builtin_amdgcn_s_sleep(GSL);
    S2_LOADS();
    HQ();
  }
  unsigned oq[5];
#pragma unroll
  for (int i = 0; i < 5; ++i) {
    const f32x2 e = unpk(x1[i]);
    oq[i] = padd(pmul(hq[i], gbf[i]), e.x, e.y);
  }
#define OUT_ROW() do { \
    __builtin_amdgcn_raw_buffer_store_b128(((u32x4){oq[0], oq[1], oq[2], oq[3]}), ro, row3 * 1280 + offA, 0, SC1); \
    __builtin_amdgcn_raw_buffer_store_b32(oq[4], ro, row3 * 1280 + offB, 0, SC1); \
  } while (0)
#define X1_PUB() do { \
    DRAIN(); \
    ST128(((u32x4){nosent(x1[0]), nosent(x1[1]), nosent(x1[2]), nosent(x1[3])}), WS_BEST + rc * 2560 + 1280 + offA); \
    ST32(nosent(x1[4]), WS_BEST + rc * 2560 + 1280 + offB); \
  } while (0)
  if (best || !is_gu) OUT_ROW();
  if (best && !is_gu) X1_PUB();

  if (is_gu) {
    unsigned kmax;
    float z0 = 0.f, z1 = 0.f;
    {
      {
    const __amdgpu_buffer_rsrc_t rd = __builtin_amdgcn_make_buffer_rsrc((void*)wd, 0, FF * 1280, 0x00020000);
    const int o = (is_gu && w < 5) ? (fb * 16 + g4 * 4) * 1280 + w * 256 + i16 * 16 : OOB;
#pragma unroll
    for (int i = 0; i < 4; ++i) dw[i] = __builtin_amdgcn_raw_buffer_load_b128(rd, o, i * 1280, 0);
      }
      SBAR();
      bf16x8 af[5];
      unsigned kk[4];
      int ko = w == 7 ? WS_KEY + xcd * 4096 + l * 16 : OOB;
#define KEY_LOAD() do { SBAR(); asm volatile("" : "+v"(ko)); _Pragma("unroll") for (int i = 0; i < 4; ++i) kk[i] = __builtin_amdgcn_raw_buffer_load_b32(rs, ko, i * 1024, SC1); SBAR(); } while (0)
      unsigned dp[2][8];
      int hbo = L_H2 + (w & 3) * 64 + g4 * 16, mro = i16 == 0 ? L_MRED + w * 64 + g4 * 16 : L_MRED + 1024;
      {
        char* stg = lds + L_WO + w * 5120;
#pragma unroll
        for (int i = 0; i < 5; ++i) *(u32x4*)(stg + i * 1024 + l * 16) = mw[i];
        CFENCE();
#pragma unroll
        for (int i = 0; i < 5; ++i) {
          const s16x4 lo = trread(stg + i * 1024 + g4 * 256 + i16 * 8), hi = trread(stg + i * 1024 + g4 * 256 + i16 * 8 + 128);
          af[i] = __builtin_shufflevector(lo, hi, 0, 1, 2, 3, 4, 5, 6, 7);
          asm volatile("" : "+v"(af[i]));
        }
#pragma unroll
        for (int p = 0; p < 2; ++p)
#pragma unroll
          for (int j = 0; j < 4; ++j) {
            dp[p][2 * j] = __builtin_amdgcn_perm(dw[2 * p + 1][j], dw[2 * p][j], 0x05040100u);
            dp[p][2 * j + 1] = __builtin_amdgcn_perm(dw[2 * p + 1][j], dw[2 * p][j], 0x07060302u);
            asm volatile("" : "+v"(dp[p][2 * j]), "+v"(dp[p][2 * j + 1]));
          }
        asm volatile("" : "+v"(hbo), "+v"(mro));
      }
      if (best) X1_PUB();
      if (w == 7) {
        int spins = 0;
        KEY_LOAD();
        for (;;) {
          kmax = __builtin_amdgcn_readfirstlane(max64(umax(umax(kk[0], kk[1]), umax(kk[2], kk[3]))));
          if (kmax != SENT || ++spins > SPIN_CAP) break;
          __builtin_amdgcn_s_sleep(3);
          KEY_LOAD();
        }
        const int sel = 2047 - (int)(kmax & 2047u);
        u32x4 r0, r1;
        int sp2 = 0;
        for (;;) {
          r0 = ld128(rs, WS_BEST + (sel >> 3) * 2560 + l * 16);
          r1 = ld128(rs, WS_BEST + (sel >> 3) * 2560 + 1024 + i16 * 16);
          if (__all(max4(r0, max4(r1, 0u)) != SENT) || ++sp2 > SPIN_CAP) break;
          __builtin_amdgcn_s_sleep(1);
        }
        *(u32x4*)(lds + L_H2 + l * 16) = r0;
        *(u32x4*)(lds + L_H2 + 1024 + i16 * 16) = r1;
      }
      LBAR();
      {
        f32x4 ga = {0.f, 0.f, 0.f, 0.f};
#pragma unroll
        for (int i = 0; i < 5; ++i) ga = __builtin_amdgcn_mfma_f32_16x16x32_bf16(af[i], *(const bf16x8*)(lds + hbo + i * 256), ga, 0, 0, 0);
        *(f32x4*)(lds + mro) = ga;
        LBAR();
        if (w < 5) {
          const float* src = (const float*)(lds + L_MRED) + g4 * 4 + (l & 3);
          const float gg = rbf((src[0] + src[16]) + (src[32] + src[48]));
          const float uu = rbf((src[64] + src[80]) + (src[96] + src[112]));
          const float sg = rbf(gg * __builtin_amdgcn_rcpf(1.0f + __expf(-gg)));
          const float act = sg * uu;
          const unsigned ap = pack2(act, dppf<0xB1>(act));
          const unsigned a01 = dppu<0x00>(ap), a23 = dppu<0xAA>(ap);
          float y[8];
#pragma unroll
          for (int j = 0; j < 8; ++j) y[j] = dot2(dp[1][j], a23, dot2(dp[0][j], a01, 0.f));
          const float u0 = rs32(y[0], y[4]), u1 = rs32(y[1], y[5]), u2 = rs32(y[2], y[6]), u3 = rs32(y[3], y[7]);
          z0 = rs16(u0, u2); z1 = rs16(u1, u3);
        }
      }
    }
    ST64(((u32x2){nosent(__float_as_uint(z0)), nosent(__float_as_uint(z1))}),
         w < 5 ? WS_YP + (w * 16 + i16) * 3456 + fb * 32 + g4 * 8 : OOB);
    if (!best) OUT_ROW();
    if (b == 0 && t == 0) {
      const u64 ne = (((u64)epv.y << 32) | epv.x) + 1ull;
      ST64(((u32x2){(unsigned)ne, (unsigned)(ne >> 32)}), WS_EPOCH);
    }
    return;
  }
  if (!is_dn) return;
  if (w == 2) {
    unsigned km;
    int spins = 0;
    __builtin_amdgcn_s_sleep(KN);
    for (;;) {
      km = 0u;
#pragma unroll
      for (int i = 0; i < 4; ++i) km = umax(km, ld32(rs, WS_KEY + xcd * 4096 + (l + 64 * i) * 16));
      if (__all(km != SENT) || ++spins > SPIN_CAP) break;
      __builtin_amdgcn_s_sleep(6);
    }
    const unsigned kmax = __builtin_amdgcn_readfirstlane(max64(km));
    const int sel = 2047 - (int)(kmax & 2047u);
    u32x4 rv;
    int sp2 = 0;
    for (;;) {
      rv = ld128(rs, WS_BEST + (sel >> 3) * 2560 + ((l >> 1) & 1) * 1280 + dnb * 32 + (l & 1) * 16);
      if (__all(max4(rv, 0u) != SENT) || ++sp2 > SPIN_CAP) break;
      __builtin_amdgcn_s_sleep(1);
    }
    if (l < 4) *(u32x4*)(lds + L_H2 + l * 16) = rv;
    if (l == 0) *(unsigned*)(lds + L_MISC + 192) = kmax;
  }
  LBAR();
  if (w < 2) {
    const int cl = w * 8 + (l & 1) * 4 + g4, col = dnb * 16 + cl;
    const unsigned kmax = *(const unsigned*)(lds + L_MISC + 192);
    float V = 0.f;
    {
      const u32x4 sb4 = *(const u32x4*)(lds + L_P3 + 1280 + offA), wr4 = *(const u32x4*)(lds + L_P3 + 4 * 1280 + offA);
#pragma unroll
      for (int i = 0; i < 4; ++i) V = dot2(sb4[i], wr4[i], V);
      V = sum64(dot2(*(const unsigned*)(lds + L_P3 + 1280 + offB), *(const unsigned*)(lds + L_P3 + 4 * 1280 + offB), V));
    }
    const unsigned ord = (kmax & ~2047u) | 1024u;
    const float rsel = rbf(__uint_as_float((ord & 0x80000000u) ? (ord & 0x7fffffffu) : ~ord) + V);
    float h2 = bf2f(*(const u16*)(lds + L_H2 + cl * 2));
    float x1v = bf2f(*(const u16*)(lds + L_H2 + 32 + cl * 2));
    float gb = bf2f(*(const u16*)(lds + L_P3 + 3 * 1280 + col * 2));
    float sg = rbf(__builtin_amdgcn_rcpf(1.0f + __expf(-rsel)));
    int odo = i16 < 2 ? ((2047 - (int)(kmax & 2047u)) * DM + col) * 2 : OOB;
    int yo = WS_YP + (dnb * 2 + w) * 3456 + l * 16, yo3 = l < 24 ? yo + 3072 : OOB;
    asm volatile("" : "+v"(h2), "+v"(x1v), "+v"(gb), "+v"(sg), "+v"(odo));
    u32x4 yv[4];
    int spins = 0;
    __builtin_amdgcn_s_sleep(YD);
    for (;;) {
      asm volatile("" : "+v"(yo), "+v"(yo3));
      yv[0] = LD128S(yo, 0); yv[1] = LD128S(yo, 1024); yv[2] = LD128S(yo, 2048); yv[3] = LD128S(yo3, 0);
      if (__all(max4(yv[0], max4(yv[1], max4(yv[2], max4(yv[3], 0u)))) != SENT) || ++spins > SPIN_CAP) break;
      __builtin_amdgcn_s_sleep(YS);
    }
    const f32x4 ys = (__builtin_bit_cast(f32x4, yv[0]) + __builtin_bit_cast(f32x4, yv[1])) + (__builtin_bit_cast(f32x4, yv[2]) + __builtin_bit_cast(f32x4, yv[3]));
    float v = rs16(rs32(ys.x, ys.z), rs32(ys.y, ys.w));
    v += dppf<0x128>(v); v += dppf<0x124>(v); v += dppf<0x122>(v);
    const float y = rbf(rbf(v) * sg);
    const float hh = rbf(h2 + y);
    __builtin_amdgcn_raw_buffer_store_b16((u16)(pack2(x1v + rbf(gb * hh), 0.f) & 0xffffu), ro, odo, 0, 16);
  }
}

extern "C" void kernel_launch(void* const* d_in, const int* in_sizes, int n_in, void* d_out, int out_size,
                              void* d_ws, size_t ws_size, hipStream_t stream) {
  k_all<<<256, 512, 0, stream>>>((const u16*)d_in[0], (const u16*)d_in[1], (const u16*)d_in[2], (const u16*)d_in[4],
                                 (const u16*)d_in[5], (const u16*)d_in[6], (const u16*)d_in[7], (const u16*)d_in[8],
                                 (const u16*)d_in[9], (char*)d_ws, (u16*)d_out);
}
```
